```python
import torch

def _softmax(x, axis=-1):
    return torch.softmax(x, dim=axis)
def _gelu(x):
    return torch.nn.functional.gelu(x, approximate="tanh")
def _silu(x):
    return torch.nn.functional.silu(x)
def _sigmoid(x):
    return torch.sigmoid(x)
def _layernorm(x, axis=-1, eps=1e-5):
    m = x.mean(dim=axis, keepdim=True)
    v = x.var(dim=axis, keepdim=True, unbiased=False)
    return (x - m) / torch.sqrt(v + eps)
def _scatter_add(tgt, idx, src, axis=0):
    return tgt.clone().index_add_(axis, idx, src)
def _segment_sum(src, seg, n):
    out = torch.zeros((n,) + src.shape[1:], dtype=src.dtype)
    return out.index_add_(0, seg, src)
def _pad(x, pw):
    flat = tuple(v for pair in reversed(pw) for v in pair)
    return torch.nn.functional.pad(x, flat)

kernel_name = 'sequential_preLN__sliding_local_dilated__swiglu_ffn__prompt'

def setup_inputs():

    _w = (lambda a: (lambda t: t.to(torch.bfloat16) if t.is_floating_point() else t)(torch.as_tensor(a)))
    def _A_setup():
        B, S, D = 1, 2048, 512
        return {
            "x": _w((lambda *s: torch.randn(*s, dtype=torch.bfloat16) * 0.05)(B, S, D)),
            "wqkv": _w((lambda *s: torch.randn(*s, dtype=torch.bfloat16) * 0.05)(D, 3 * D)),
            "wo": _w((lambda *s: torch.randn(*s, dtype=torch.bfloat16) * 0.05)(D, D)),
        }
    _A_in = _A_setup()

    def _B_setup():
        B, S, D, F = 1, 2048, 512, 1792
        return {
            "x": _w((lambda *s: torch.randn(*s, dtype=torch.bfloat16) * 0.05)(B, S, D)),
            "w_gate": _w((lambda *s: torch.randn(*s, dtype=torch.bfloat16) * 0.05)(D, F)),
            "w_up": _w((lambda *s: torch.randn(*s, dtype=torch.bfloat16) * 0.05)(D, F)),
            "w_down": _w((lambda *s: torch.randn(*s, dtype=torch.bfloat16) * 0.05)(F, D)),
        }
    _B_in = _B_setup()

    return {
        "A_x": _A_in["x"],
        "A_wqkv": _A_in["wqkv"],
        "A_wo": _A_in["wo"],
        "B_x": _B_in["x"],
        "B_w_gate": _B_in["w_gate"],
        "B_w_up": _B_in["w_up"],
        "B_w_down": _B_in["w_down"],
    }

def reference(A_x, A_wqkv, A_wo, B_x, B_w_gate, B_w_up, B_w_down):
    def _A_ref(x, wqkv, wo):
        B, S, D = x.shape
        H = 32; Dh = D // H
        W, dil = 8, 2
        qkv = (lambda x, *d: x.permute(*d))((x @ wqkv).reshape(B, S, 3, H, Dh), 2, 0, 3, 1, 4)
        q, k, v = qkv[0], qkv[1], qkv[2]
        scale = Dh ** -0.5
        pos = torch.arange(S)
        off = torch.arange(W) * dil
        idx = pos[:, None] - off[None, :]
        mask = idx >= 0
        idx_c = torch.where(mask, idx, 0).reshape(-1)
        kg = k[:, :, idx_c, :].reshape(B, H, S, W, Dh)
        vg = v[:, :, idx_c, :].reshape(B, H, S, W, Dh)
        logits = torch.einsum("bhsd,bhswd->bhsw", q, kg) * scale
        logits = torch.where(mask[None, None, :, :], logits, -1e9)
        attn = _softmax(logits, axis=-1)
        o = torch.einsum("bhsw,bhswd->bhsd", attn, vg)
        return (lambda x, *d: x.permute(*d))(o, 0, 2, 1, 3).reshape(B, S, D) @ wo

    def _B_ref(x, w_gate, w_up, w_down):
        g = _silu(x @ w_gate)
        u = x @ w_up
        return (g * u) @ w_down

    x = A_x
    h = x + _A_ref(_layernorm(x, axis=-1), A_wqkv, A_wo)
    h2 = h + _B_ref(_layernorm(h, axis=-1), B_w_gate, B_w_up, B_w_down)
    return h2
```

```cpp
#include <hip/hip_runtime.h>
#include <hip/hip_bf16.h>
typedef unsigned short u16;
__device__ __forceinline__ float bf2f(u16 v){ return __uint_as_float(((unsigned)v)<<16); }
__device__ __forceinline__ u16 f2bf(float f){ __hip_bfloat16 b = __float2bfloat16(f); return *(u16*)&b; }
__device__ __forceinline__ float rbf(float f){ return bf2f(f2bf(f)); }

__global__ void ln_k(const u16* x, u16* y){
  int row = blockIdx.x; int l = threadIdx.x;
  float v[8]; float s=0;
  for(int i=0;i<8;i++){ v[i]=bf2f(x[row*512+l*8+i]); s+=v[i]; }
  for(int o=32;o>0;o>>=1) s+=__shfl_xor(s,o);
  float m=s/512.f; float q=0;
  for(int i=0;i<8;i++){ float d=v[i]-m; q+=d*d; }
  for(int o=32;o>0;o>>=1) q+=__shfl_xor(q,o);
  float var=rbf(q/512.f); float mb=rbf(m);
  float sd=rbf(sqrtf(rbf(var+1e-5f)));
  for(int i=0;i<8;i++) y[row*512+l*8+i]=f2bf(rbf(v[i]-mb)/sd);
}
__global__ void gemm_k(const u16* A,const u16* B,u16* C,const u16* res,int M,int N,int K){
  int n=blockIdx.x*64+threadIdx.x; int m=blockIdx.y;
  float acc=0;
  for(int k=0;k<K;k++) acc+=bf2f(A[m*K+k])*bf2f(B[k*N+n]);
  if(res) acc=bf2f(res[m*N+n])+rbf(acc);
  C[m*N+n]=f2bf(acc);
}
__global__ void attn_k(const u16* qkv,u16* o){
  int t=blockIdx.x*blockDim.x+threadIdx.x; int h=t&31, s=t>>5;
  float q[16]; for(int d=0;d<16;d++) q[d]=bf2f(qkv[s*1536+h*16+d]);
  float lg[8]; float mx=-3e38f;
  for(int w=0;w<8;w++){ int i=s-2*w; float a;
    if(i>=0){ a=0; for(int d=0;d<16;d++) a+=q[d]*bf2f(qkv[i*1536+512+h*16+d]); a=rbf(rbf(a)*0.25f);} else a=rbf(-1e9f);
    lg[w]=a; mx=fmaxf(mx,a);}
  float sum=0; for(int w=0;w<8;w++){ lg[w]=__expf(lg[w]-mx); sum+=lg[w]; }
  float oo[16]; for(int d=0;d<16;d++) oo[d]=0;
  for(int w=0;w<8;w++){ int i=s-2*w; if(i<0) i=0; float p=rbf(lg[w]/sum); for(int d=0;d<16;d++) oo[d]+=p*bf2f(qkv[i*1536+1024+h*16+d]); }
  for(int d=0;d<16;d++) o[s*512+h*16+d]=f2bf(oo[d]);
}
__global__ void act_k(const u16* g,const u16* u,u16* a,int n){ int i=blockIdx.x*blockDim.x+threadIdx.x; if(i<n){ float x=bf2f(g[i]); float s=rbf(x/(1.f+__expf(-x))); a[i]=f2bf(s*bf2f(u[i])); } }

extern "C" void kernel_launch(void* const* d_in, const int* in_sizes, int n_in, void* d_out, int out_size, void* d_ws, size_t ws_size, hipStream_t stream){
  const u16* x=(const u16*)d_in[0]; const u16* wqkv=(const u16*)d_in[1]; const u16* wo=(const u16*)d_in[2];
  const u16* wg=(const u16*)d_in[4]; const u16* wu=(const u16*)d_in[5]; const u16* wd=(const u16*)d_in[6];
  u16* ws=(u16*)d_ws; u16* xn=ws; u16* qkv=xn+2048*512; u16* o=qkv+2048*1536; u16* h=o+2048*512; u16* hn=h+2048*512; u16* g=hn+2048*512; u16* u=g+2048*1792; u16* a=u+2048*1792;
  ln_k<<<2048,64,0,stream>>>(x,xn);
  gemm_k<<<dim3(1536/64,2048),64,0,stream>>>(xn,wqkv,qkv,nullptr,2048,1536,512);
  attn_k<<<2048*32/256,256,0,stream>>>(qkv,o);
  gemm_k<<<dim3(512/64,2048),64,0,stream>>>(o,wo,h,x,2048,512,512);
  ln_k<<<2048,64,0,stream>>>(h,hn);
  gemm_k<<<dim3(1792/64,2048),64,0,stream>>>(hn,wg,g,nullptr,2048,1792,512);
  gemm_k<<<dim3(1792/64,2048),64,0,stream>>>(hn,wu,u,nullptr,2048,1792,512);
  act_k<<<2048*1792/256,256,0,stream>>>(g,u,a,2048*1792);
  gemm_k<<<dim3(512/64,2048),64,0,stream>>>(a,wd,(u16*)d_out,h,2048,512,1792);
}
```

```cpp
#include <hip/hip_runtime.h>
#include <hip/hip_bf16.h>

#define NLAUNCH 1

typedef unsigned short u16;
typedef unsigned long long u64;
typedef __attribute__((ext_vector_type(8))) __bf16 bf16x8;
typedef __attribute__((ext_vector_type(2))) __bf16 bf16x2;
typedef __attribute__((ext_vector_type(4))) float f32x4;
typedef __attribute__((ext_vector_type(2))) float f32x2;
typedef __attribute__((ext_vector_type(4))) short s16x4;
typedef __attribute__((ext_vector_type(8))) short s16x8;
typedef __attribute__((ext_vector_type(4))) unsigned u32x4;
typedef __attribute__((ext_vector_type(2))) unsigned u32x2;
#define LDSP __attribute__((address_space(3)))
#define GLB __attribute__((address_space(1)))
#define DI __device__ __forceinline__
typedef __amdgpu_buffer_rsrc_t rsrc_t;

constexpr int S_ = 2048, D_ = 512, F_ = 1792;
constexpr unsigned WS_EPOCH = 0;
constexpr unsigned WS_F1 = 4096;
constexpr unsigned WS_F2 = WS_F1 + 4096;
constexpr unsigned WS_F3 = WS_F2 + 4096;
constexpr unsigned WS_O = 65536;
constexpr unsigned WS_H = WS_O + 2097152;
constexpr unsigned WS_A = WS_H + 2097152;
constexpr unsigned WS_END = WS_A + 128 * 56 * 1024;
constexpr unsigned SLOT = 34816;
constexpr unsigned L_SP = 4 * SLOT;
constexpr unsigned L_ST = L_SP;
constexpr unsigned L_CS = L_SP + 1024;
constexpr unsigned L_RED = L_SP + 2048;
constexpr unsigned L_CS3 = L_SP + 2048 + 16384;
constexpr unsigned L_UP = 2 * SLOT;
constexpr unsigned L_GATE = 0;
constexpr unsigned LDS_BYTES = 163840;

extern __shared__ __attribute__((aligned(16))) char smem[];
#define TS(i)

DI unsigned rfl(unsigned v) { return __builtin_amdgcn_readfirstlane(v); }
DI rsrc_t mk(const void* p, unsigned bytes) { return __builtin_amdgcn_make_buffer_rsrc((void*)p, (short)0, (int)bytes, 0x00020000); }
DI void dma(unsigned lds, unsigned voff, rsrc_t r, unsigned soff) {
  asm volatile("s_nop 4\n\ts_mov_b32 m0, %0\n\ts_nop 0\n\tbuffer_load_dwordx4 %1, %2, %3 offen lds"
               :: "s"(rfl(lds + (unsigned)(size_t)(LDSP char*)smem)), "v"(voff), "s"(r), "s"(rfl(soff)) : "memory");
}
template <int N> DI void wait_vm() { asm volatile("s_waitcnt vmcnt(%0)" :: "n"(N) : "memory"); }
DI void bar() { asm volatile("s_waitcnt lgkmcnt(0)\n\ts_barrier" ::: "memory"); }
DI bf16x8 lds_b128(unsigned a) { return *(LDSP bf16x8*)(smem + a); }
DI bf16x8 lds_tr2(unsigned a0, unsigned a1) {
  s16x4 lo = __builtin_amdgcn_ds_read_tr16_b64_v4i16((LDSP s16x4*)(smem + a0));
  s16x4 hi = __builtin_amdgcn_ds_read_tr16_b64_v4i16((LDSP s16x4*)(smem + a1));
  s16x8 v = __builtin_shufflevector(lo, hi, 0, 1, 2, 3, 4, 5, 6, 7);
  return __builtin_bit_cast(bf16x8, v);
}
DI f32x4 mfma32(bf16x8 a, bf16x8 b, f32x4 c) { return __builtin_amdgcn_mfma_f32_16x16x32_bf16(a, b, c, 0, 0, 0); }
DI f32x4 mfma16(u32x2 a, u32x2 b, f32x4 c) {
  return __builtin_amdgcn_mfma_f32_16x16x16bf16_1k(__builtin_bit_cast(s16x4, a), __builtin_bit_cast(s16x4, b), c, 0, 0, 0);
}
DI unsigned pk(float a, float b) { f32x2 f = {a, b}; return __builtin_bit_cast(unsigned, __builtin_convertvector(f, bf16x2)); }
DI float rbf(float a) { return __uint_as_float(pk(a, a) << 16); }
DI float blo(unsigned v) { return __uint_as_float(v << 16); }
DI float bhi(unsigned v) { return __uint_as_float(v & 0xffff0000u); }
DI float dot2(unsigned a, unsigned b, float c) {
  return __builtin_amdgcn_fdot2_f32_bf16(__builtin_bit_cast(bf16x2, a), __builtin_bit_cast(bf16x2, b), c, false);
}
DI float sum4rows(float v) {
  auto a = __builtin_amdgcn_permlane16_swap(__float_as_uint(v), __float_as_uint(v), false, false);
  float s = __uint_as_float(a[0]) + __uint_as_float(a[1]);
  auto b = __builtin_amdgcn_permlane32_swap(__float_as_uint(s), __float_as_uint(s), false, false);
  return __uint_as_float(b[0]) + __uint_as_float(b[1]);
}
DI float max4rows(float v) {
  auto a = __builtin_amdgcn_permlane16_swap(__float_as_uint(v), __float_as_uint(v), false, false);
  float s = fmaxf(__uint_as_float(a[0]), __uint_as_float(a[1]));
  auto b = __builtin_amdgcn_permlane32_swap(__float_as_uint(s), __float_as_uint(s), false, false);
  return fmaxf(__uint_as_float(b[0]), __uint_as_float(b[1]));
}
DI u32x4 swap_pair(unsigned a0, unsigned a1, unsigned b0, unsigned b1) {
  auto r0 = __builtin_amdgcn_permlane16_swap(a0, b0, false, false);
  auto r1 = __builtin_amdgcn_permlane16_swap(a1, b1, false, false);
  u32x4 v = {r0[0], r1[0], r0[1], r1[1]};
  return v;
}
DI f32x2 ln_stats(float s1, float s2) {
  float m = s1 * (1.f / 512.f);
  float var = fmaxf(s2 * (1.f / 512.f) - m * m, 0.f);
  float sd = rbf(__builtin_amdgcn_sqrtf(rbf(rbf(var) + 1e-5f)));
  f32x2 r = {rbf(m), __builtin_amdgcn_rcpf(sd)};
  return r;
}
DI void publish(char* ws, unsigned off, u64 tag, int tid) {
  asm volatile("s_waitcnt vmcnt(0)" ::: "memory");
  bar();
  if (tid == 0) __hip_atomic_store((GLB u64*)(ws + off), tag, __ATOMIC_RELAXED, __HIP_MEMORY_SCOPE_AGENT);
}
DI void poll(char* ws, unsigned off, int n, u64 tag, int lane) {
  GLB u64* p = (GLB u64*)(ws + off) + (lane < n ? lane : 0);
  for (int spin = 0; spin < (1 << 20); ++spin) {
    u64 v = __hip_atomic_load(p, __ATOMIC_RELAXED, __HIP_MEMORY_SCOPE_AGENT);
    if (__all(v == tag)) break;
    __builtin_amdgcn_s_sleep(1);
  }
  __builtin_amdgcn_fence(__ATOMIC_ACQUIRE, "wavefront");
  asm volatile("" ::: "memory");
}

template <int LO, int HI>
__global__ __launch_bounds__(512, 2) void fused_block(const u16* __restrict__ x, const u16* __restrict__ wqkv,
                                                      const u16* __restrict__ wo, const u16* __restrict__ wg,
                                                      const u16* __restrict__ wu, const u16* __restrict__ wd,
                                                      u16* __restrict__ out, char* __restrict__ ws) {
  constexpr bool ONE = (LO == 1 && HI == 4);
  const int tid = threadIdx.x, lane = tid & 63;
  const unsigned w = rfl(tid >> 6);
  const unsigned l15 = lane & 15, g = lane >> 4, q = (lane >> 2) & 3, p4 = lane & 3;
  const unsigned b = blockIdx.x, xcd = b & 7, bi = b >> 3;
  const rsrc_t rWS = mk(ws, WS_END);
  u64 tag = 0, epoch = 0;
  TS(0);
  if (ONE) {
    epoch = __hip_atomic_load((GLB u64*)(ws + WS_EPOCH), __ATOMIC_RELAXED, __HIP_MEMORY_SCOPE_AGENT);
  }
  const unsigned rt = xcd * 4 + (bi & 3), cg = bi >> 2;
  const unsigned panel = (xcd >> 1) * 2 + (bi & 1), fg = (xcd & 1) * 14 + (bi >> 1);
  const bool p3on = bi < 28;
  const unsigned prow = lane >> 3, pp = lane & 7;
  const unsigned fN = ((prow >> 1) & 1) | ((w & 1) << 1);
  const unsigned swzN = (((pp >> 1) ^ fN) << 5) | ((pp & 1) << 4);
  const unsigned swzP = (((pp >> 1) ^ ((prow >> 1) & 3)) << 5) | ((pp & 1) << 4);
  const unsigned frN = ((q >> 1) & 1) | ((g & 1) << 1);
  const unsigned trowN = (8 * g + q) * 128 + p4 * 8;
  const unsigned frP = ((4 * g + q) >> 1) & 3;
  const unsigned trowP = (4 * g + q) * 128 + p4 * 8;

  auto dma_wo = [&](int kh) {
    const rsrc_t r = mk(wo, D_ * D_ * 2);
    const unsigned v = (8 * w + prow) * 1024 + cg * 128 + swzN;
#pragma unroll
    for (int j = 0; j < 4; ++j) dma(kh * SLOT + (w + 8 * j) * 1024, v, r, (256 * kh + 64 * j) * 1024);
  };
  auto dma_w3 = [&](const u16* wm, unsigned base) {
    const rsrc_t r = mk(wm, D_ * F_ * 2);
    const unsigned v = (8 * w + prow) * (F_ * 2) + fg * 128 + swzN;
#pragma unroll
    for (int j = 0; j < 8; ++j) dma(base + (w + 8 * j) * 1024, v, r, 64 * j * (F_ * 2));
  };
  auto dma_wd = [&](int i) {
    const rsrc_t r = mk(wd, D_ * F_ * 2);
    const unsigned v = (8 * (w & 3) + prow) * 1024 + cg * 128 + swzP;
#pragma unroll
    for (int j = 0; j < 2; ++j) {
      const unsigned kq = (w >> 2) + 2 * j;
      dma((kq * 9 + (i % 9)) * 4096 + (w & 3) * 1024, v, r, (kq * 14 + i) * 32 * 1024);
    }
  };

  u32x4 hreg = {0, 0, 0, 0};

  if (LO <= 1) {
    const unsigned rg = (xcd >> 1) * 8 + (bi & 7), hg = (xcd & 1) * 4 + (bi >> 3);
    const unsigned rot = ((bi & 7) + 2 * (bi >> 3)) & 7;
    const rsrc_t rX = mk(x, S_ * D_ * 2), rW = mk(wqkv, D_ * 1536 * 2);
    const int r0 = 8 * w + prow, r1 = 64 + 8 * (w & 1) + prow;
    const unsigned vX0 = max((int)rg * 64 - 16 + r0, 0) * 1024 + ((pp ^ ((r0 >> 1) & 7)) << 4);
    const unsigned vX1 = (rg * 64 - 16 + r1) * 1024 + ((pp ^ ((r1 >> 1) & 7)) << 4);
    const unsigned vW = (8 * w + prow) * 3072 + hg * 128 + swzN;
    auto issue = [&](int s) {
      const unsigned st = (s + rot) & 7, sb = (s & 3) * SLOT;
      if (w < 2) dma(sb + 8192 + w * 1024, vX1, rX, st * 128);
      if (tid >= 8) dma(sb + w * 1024, vX0, rX, st * 128);
#pragma unroll
      for (int m = 0; m < 3; ++m) dma(sb + 10240 + m * 8192 + w * 1024, vW, rW, st * 64 * 3072 + m * 1024);
    };
    issue(0); issue(1); issue(2);
    if (tid < 32) { u32x4 ones = {0x3f803f80u, 0x3f803f80u, 0x3f803f80u, 0x3f803f80u};
      *(LDSP u32x4*)(smem + (tid >> 3) * SLOT + (tid & 7) * 16) = ones; }
    const unsigned hw = w & 3, half = w >> 2;
    const unsigned xo0 = l15 * 128 + ((g ^ (l15 >> 1)) << 4);
    const unsigned xa = xo0 + half * 4096, xs = xo0 + (w < 5 ? w : 0) * 2048;
    const unsigned wa = 10240 + trowN + ((hw ^ frN) << 5);
    f32x4 aQ[3], aK[3], aV[3];
#pragma unroll
    for (int i = 0; i < 3; ++i) { aQ[i] = {0, 0, 0, 0}; aK[i] = {0, 0, 0, 0}; aV[i] = {0, 0, 0, 0}; }
    float s1 = 0.f, s2 = 0.f;
#pragma unroll
    for (int s = 0; s < 8; ++s) {
      if (s <= 5) { if (w < 2) wait_vm<10>(); else wait_vm<8>(); }
      else if (s == 6) { if (HI >= 2) { if (w < 2) wait_vm<9>(); else wait_vm<8>(); } else { if (w < 2) wait_vm<5>(); else wait_vm<4>(); } }
      else { if (HI >= 2) wait_vm<8>(); else wait_vm<0>(); }
      bar();
      if (s == 0) TS(1);
      if (s == 4) TS(2);
      if (s + 3 < 8) issue(s + 3);
      else if (HI >= 2 && s < 7) dma_wo(s - 5);
      const unsigned sb = (s & 3) * SLOT;
#pragma unroll
      for (int kk = 0; kk < 2; ++kk) {
        bf16x8 xf[3], wf[3];
#pragma unroll
        for (int t = 0; t < 3; ++t) xf[t] = lds_b128(sb + ((xa ^ (kk * 64)) + t * 2048));
#pragma unroll
        for (int m = 0; m < 3; ++m) wf[m] = lds_tr2(sb + wa + m * 8192 + kk * 4096, sb + wa + m * 8192 + kk * 4096 + 512);
        if (w < 5) {
          u32x4 sx = __builtin_bit_cast(u32x4, lds_b128(sb + (xs ^ (kk * 64))));
#pragma unroll
          for (int j = 0; j < 4; ++j) { s1 = dot2(sx[j], 0x3f803f80u, s1); s2 = dot2(sx[j], sx[j], s2); }
        }
        if (half == 0) aQ[0] = mfma32(wf[0], xf[0], aQ[0]);
        aQ[1] = mfma32(wf[0], xf[1], aQ[1]);
        aQ[2] = mfma32(wf[0], xf[2], aQ[2]);
#pragma unroll
        for (int t = 0; t < 3; ++t) { aK[t] = mfma32(wf[1], xf[t], aK[t]); aV[t] = mfma32(xf[t], wf[2], aV[t]); }
      }
    }
    TS(3);
    if (w < 5) {
      f32x2 st = ln_stats(sum4rows(s1), sum4rows(s2));
      if (g == 0) *(LDSP f32x2*)(smem + L_ST + (w * 16 + l15) * 8) = st;
    }
    if (half == 0) {
      if (l15 == 0) { *(LDSP f32x4*)(smem + L_CS + (hw * 3 + 0) * 64 + g * 16) = aQ[0];
                      *(LDSP f32x4*)(smem + L_CS + (hw * 3 + 1) * 64 + g * 16) = aK[0]; }
      if (g == 0) *(LDSP float*)(smem + L_CS + (hw * 3 + 2) * 64 + l15 * 4) = aV[0][0];
    }
    bar();
    const f32x4 csq = *(LDSP f32x4*)(smem + L_CS + (hw * 3 + 0) * 64 + g * 16);
    const f32x4 csk = *(LDSP f32x4*)(smem + L_CS + (hw * 3 + 1) * 64 + g * 16);
    const float csv = *(LDSP float*)(smem + L_CS + (hw * 3 + 2) * 64 + l15 * 4);
    u32x2 qb[3], kb[3], vb[3];
#pragma unroll
    for (int t = 0; t < 3; ++t) {
      const unsigned T = half * 2 + t;
      const f32x2 st = *(LDSP f32x2*)(smem + L_ST + (T * 16 + l15) * 8);
      const f32x4 sa = *(LDSP f32x4*)(smem + L_ST + (T * 16 + 4 * g) * 8);
      const f32x4 sb2 = *(LDSP f32x4*)(smem + L_ST + (T * 16 + 4 * g) * 8 + 16);
      float qv[4], kv[4];
#pragma unroll
      for (int r = 0; r < 4; ++r) { qv[r] = st[1] * (aQ[t][r] - st[0] * csq[r]); kv[r] = st[1] * (aK[t][r] - st[0] * csk[r]); }
      qb[t] = {pk(qv[0], qv[1]), pk(qv[2], qv[3])};
      kb[t] = {pk(kv[0], kv[1]), pk(kv[2], kv[3])};
      vb[t] = {pk(sa[1] * (aV[t][0] - sa[0] * csv), sa[3] * (aV[t][1] - sa[2] * csv)),
               pk(sb2[1] * (aV[t][2] - sb2[0] * csv), sb2[3] * (aV[t][3] - sb2[2] * csv))};
    }
    unsigned ob[2][2];
    const unsigned par = l15 & 1;
#pragma unroll
    for (int t = 1; t < 3; ++t) {
      const f32x4 z = {0, 0, 0, 0};
      const f32x4 sc = mfma16(kb[t], qb[t], z), sp = mfma16(kb[t - 1], qb[t], z);
      const bool nop = (rg == 0 && half == 0 && t == 1);
      const bool c0 = (4 * g + par) <= l15, c1 = (4 * g + 2 + par) <= l15;
      float l0 = c0 ? (par ? sc[1] : sc[0]) : (par ? sp[1] : sp[0]);
      float l1 = c1 ? (par ? sc[3] : sc[2]) : (par ? sp[3] : sp[2]);
      l0 = rbf(rbf(l0) * 0.25f); l1 = rbf(rbf(l1) * 0.25f);
      if (nop && !c0) l0 = -1e9f;
      if (nop && !c1) l1 = -1e9f;
      const float mx = max4rows(fmaxf(l0, l1));
      const float e0 = __builtin_amdgcn_exp2f((l0 - mx) * 1.44269504f), e1 = __builtin_amdgcn_exp2f((l1 - mx) * 1.44269504f);
      const float inv = __builtin_amdgcn_rcpf(sum4rows(e0 + e1));
      const float p0 = e0 * inv, p1 = e1 * inv;
      const unsigned w0 = par ? pk(0.f, p0) : pk(p0, 0.f), w1 = par ? pk(0.f, p1) : pk(p1, 0.f);
      u32x4 pf = {c0 ? 0u : w0, c1 ? 0u : w1, c0 ? w0 : 0u, c1 ? w1 : 0u};
      u32x4 vf = {vb[t - 1][0], vb[t - 1][1], vb[t][0], vb[t][1]};
      const f32x4 o = mfma32(__builtin_bit_cast(bf16x8, vf), __builtin_bit_cast(bf16x8, pf), z);
      ob[t - 1][0] = pk(o[0], o[1]); ob[t - 1][1] = pk(o[2], o[3]);
    }
    {
      const u32x4 ov = swap_pair(ob[0][0], ob[0][1], ob[1][0], ob[1][1]);
      const unsigned head = hg * 4 + hw;
      const unsigned rtile = rg * 4 + half * 2 + (g & 1);
      const unsigned off = WS_O + (rtile * 16 + (head >> 1)) * 1024 + (((head & 1) * 2 + (g >> 1)) * 16 + l15) * 16;
      __builtin_amdgcn_raw_buffer_store_b128(ov, rWS, off, 0, 16);
    }
    TS(4);
    if (ONE) {
      tag = epoch * 0x9E3779B97F4A7C15ull + 0x632BE59BD9B4E019ull;
      publish(ws, WS_F1 + (rg * 16 + hg) * 8, tag, tid);
      TS(5);
      if (p3on) dma_w3(wu, L_UP);
    }
  }

  if (LO <= 2 && HI >= 2) {
    const unsigned m = w & 3, kh = w >> 2;
    if (!ONE) { dma_wo(0); dma_wo(1); wait_vm<0>(); bar(); }
    const unsigned gp = ((g & 1) << 1) | (g >> 1);
    const u32x4 xr = __builtin_amdgcn_raw_buffer_load_b128(mk(x, S_ * D_ * 2), (rt * 64 + m * 16 + l15) * 1024 + cg * 128 + kh * 64 + gp * 16, 0, 0);
    bf16x8 wf[8][4];
#pragma unroll
    for (int ks = 0; ks < 8; ++ks)
#pragma unroll
      for (int c = 0; c < 4; ++c) {
        const unsigned a = kh * SLOT + trowN + ((c ^ frN) << 5) + ks * 4096;
        wf[ks][c] = lds_tr2(a, a + 512);
      }
    if (ONE) {
      bar();
      if (p3on) dma_w3(wg, L_GATE);
      TS(6);
      poll(ws, WS_F1 + (rt * 16 + kh * 4) * 8, 4, tag, lane);
      TS(7);
    }
    u32x4 of[8];
#pragma unroll
    for (int ks = 0; ks < 8; ++ks)
      of[ks] = __builtin_amdgcn_raw_buffer_load_b128(rWS, WS_O + lane * 16 + ks * 1024, ((rt * 4 + m) * 16 + kh * 8) * 1024, 16);
    f32x4 acc[4];
#pragma unroll
    for (int c = 0; c < 4; ++c) acc[c] = {0, 0, 0, 0};
#pragma unroll
    for (int ks = 0; ks < 8; ++ks)
#pragma unroll
      for (int c = 0; c < 4; ++c) acc[c] = mfma32(wf[ks][c], __builtin_bit_cast(bf16x8, of[ks]), acc[c]);
    TS(8);
    const f32x4 give0 = kh ? acc[0] : acc[2], give1 = kh ? acc[1] : acc[3];
    f32x4 k0 = kh ? acc[2] : acc[0], k1 = kh ? acc[3] : acc[1];
    *(LDSP f32x4*)(smem + L_RED + w * 2048 + lane * 16) = give0;
    *(LDSP f32x4*)(smem + L_RED + w * 2048 + 1024 + lane * 16) = give1;
    bar();
    k0 += *(LDSP f32x4*)(smem + L_RED + (w ^ 4) * 2048 + lane * 16);
    k1 += *(LDSP f32x4*)(smem + L_RED + (w ^ 4) * 2048 + 1024 + lane * 16);
    const u32x4 av = swap_pair(pk(k0[0], k0[1]), pk(k0[2], k0[3]), pk(k1[0], k1[1]), pk(k1[2], k1[3]));
#pragma unroll
    for (int j = 0; j < 4; ++j) hreg[j] = pk(blo(av[j]) + blo(xr[j]), bhi(av[j]) + bhi(xr[j]));
    __builtin_amdgcn_raw_buffer_store_b128(hreg, rWS, WS_H + ((rt * 4 + m) * 16 + cg * 2 + kh) * 1024 + (gp * 16 + l15) * 16, 0, 16);
    if (ONE) publish(ws, WS_F2 + (rt * 16 + cg) * 8, tag, tid);
    TS(9);
  }

  if (LO <= 3 && HI >= 3) {
    if (p3on) {
      if (!ONE) { dma_w3(wg, L_GATE); dma_w3(wu, L_UP); wait_vm<0>(); bar(); }
      unsigned ta[4];
#pragma unroll
      for (int c = 0; c < 4; ++c) ta[c] = trowN + ((c ^ frN) << 5);
      {
        const unsigned a = (w >> 2) * L_UP + trowN + (((w & 3) ^ frN) << 5);
        const u32x4 ones = {0x3f803f80u, 0x3f803f80u, 0x3f803f80u, 0x3f803f80u};
        f32x4 cs = {0, 0, 0, 0};
#pragma unroll
        for (int ks = 0; ks < 16; ++ks) cs = mfma32(lds_tr2(a + ks * 4096, a + ks * 4096 + 512), __builtin_bit_cast(bf16x8, ones), cs);
        if (l15 == 0) *(LDSP f32x4*)(smem + L_CS3 + w * 64 + g * 16) = cs;
        bar();
      }
      TS(10);
      if (ONE) poll(ws, WS_F2 + ((panel * 4 + (w >> 1)) * 16) * 8, 8, tag, lane);
      TS(11);
      const unsigned rot = (bi >> 1) & 15;
      const unsigned hb = WS_H + (panel * 16 + 2 * w) * 16 * 1024;
      f32x4 aG[2][4], aU[2][4];
#pragma unroll
      for (int t = 0; t < 2; ++t)
#pragma unroll
        for (int c = 0; c < 4; ++c) { aG[t][c] = {0, 0, 0, 0}; aU[t][c] = {0, 0, 0, 0}; }
      float s1[2] = {0.f, 0.f}, s2[2] = {0.f, 0.f};
      constexpr int PF = 8;
      u32x4 hf[16][2];
      auto ldh = [&](int kk) {
        const unsigned ks = (kk + rot) & 15;
        hf[kk][0] = __builtin_amdgcn_raw_buffer_load_b128(rWS, lane * 16, hb + ks * 1024, 16);
        hf[kk][1] = __builtin_amdgcn_raw_buffer_load_b128(rWS, lane * 16, hb + 16384 + ks * 1024, 16);
      };
#pragma unroll
      for (int kk = 0; kk < PF; ++kk) ldh(kk);
#pragma unroll
      for (int kk = 0; kk < 16; ++kk) {
        if (kk + PF < 16) ldh(kk + PF);
        const unsigned ko = ((kk + rot) & 15) * 4096;
        bf16x8 gf[4], uf[4];
#pragma unroll
        for (int c = 0; c < 4; ++c) { gf[c] = lds_tr2(L_GATE + ko + ta[c], L_GATE + ko + ta[c] + 512);
                                      uf[c] = lds_tr2(L_UP + ko + ta[c], L_UP + ko + ta[c] + 512); }
#pragma unroll
        for (int t = 0; t < 2; ++t) {
#pragma unroll
          for (int j = 0; j < 4; ++j) { s1[t] = dot2(hf[kk][t][j], 0x3f803f80u, s1[t]); s2[t] = dot2(hf[kk][t][j], hf[kk][t][j], s2[t]); }
#pragma unroll
          for (int c = 0; c < 4; ++c) { aG[t][c] = mfma32(gf[c], __builtin_bit_cast(bf16x8, hf[kk][t]), aG[t][c]);
                                        aU[t][c] = mfma32(uf[c], __builtin_bit_cast(bf16x8, hf[kk][t]), aU[t][c]); }
        }
      }
      TS(12);
      f32x4 csg[4], csu[4];
#pragma unroll
      for (int c = 0; c < 4; ++c) { csg[c] = *(LDSP f32x4*)(smem + L_CS3 + c * 64 + g * 16); csu[c] = *(LDSP f32x4*)(smem + L_CS3 + (4 + c) * 64 + g * 16); }
#pragma unroll
      for (int t = 0; t < 2; ++t) {
        const f32x2 st = ln_stats(sum4rows(s1[t]), sum4rows(s2[t]));
        unsigned av[4][2];
#pragma unroll
        for (int c = 0; c < 4; ++c) {
          float r[4];
#pragma unroll
          for (int j = 0; j < 4; ++j) {
            const float gg = rbf(st[1] * (aG[t][c][j] - st[0] * csg[c][j]));
            const float uu = rbf(st[1] * (aU[t][c][j] - st[0] * csu[c][j]));
            const float sg = rbf(gg * __builtin_amdgcn_rcpf(1.f + __builtin_amdgcn_exp2f(gg * -1.44269504f)));
            r[j] = sg * uu;
          }
          av[c][0] = pk(r[0], r[1]); av[c][1] = pk(r[2], r[3]);
        }
#pragma unroll
        for (int cp = 0; cp < 2; ++cp) {
          const u32x4 v = {av[2 * cp][0], av[2 * cp][1], av[2 * cp + 1][0], av[2 * cp + 1][1]};
          __builtin_amdgcn_raw_buffer_store_b128(v, rWS, lane * 16, WS_A + ((panel * 16 + 2 * w + t) * 56 + fg * 2 + cp) * 1024, 16);
        }
      }
      TS(13);
      if (ONE) publish(ws, WS_F3 + (panel * 32 + fg) * 8, tag, tid);
      TS(14);
    } else if (ONE && b == 255 && w == 0) {
#pragma unroll
      for (int j = 0; j < 4; ++j) {
        GLB u64* p = (GLB u64*)(ws + WS_F1) + (j * 8 + (lane >> 3)) * 16 + (lane & 7);
        for (int spin = 0; spin < (1 << 20); ++spin) {
          u64 v = __hip_atomic_load(p, __ATOMIC_RELAXED, __HIP_MEMORY_SCOPE_AGENT);
          if (__all(v == tag)) break;
          __builtin_amdgcn_s_sleep(1);
        }
      }
      if (lane == 0) __hip_atomic_store((GLB u64*)(ws + WS_EPOCH), epoch + 1, __ATOMIC_RELAXED, __HIP_MEMORY_SCOPE_AGENT);
    }
  }

  if (HI >= 4) {
    const unsigned mh = (w >> 1) & 1, kq = (w & 1) + 2 * (w >> 2);
    const unsigned gp = ((g & 1) << 1) | (g >> 1);
#pragma unroll
    for (int i = 0; i < 9; ++i) dma_wd(i);
    if (!ONE) hreg = __builtin_amdgcn_raw_buffer_load_b128(rWS, WS_H + ((rt * 4 + (w & 3)) * 16 + cg * 2 + (w >> 2)) * 1024 + (gp * 16 + l15) * 16, 0, 0);
    if (ONE) poll(ws, WS_F3 + ((rt >> 2) * 32 + kq * 7) * 8, 7, tag, lane);
    TS(15);
    u32x4 af[14][2];
    const unsigned ab = WS_A + ((rt * 4 + 2 * mh) * 56 + kq * 14) * 1024;
#pragma unroll
    for (int i = 0; i < 14; ++i) {
      af[i][0] = __builtin_amdgcn_raw_buffer_load_b128(rWS, lane * 16 + i * 1024, ab, 16);
      af[i][1] = __builtin_amdgcn_raw_buffer_load_b128(rWS, lane * 16 + i * 1024, ab + 56 * 1024, 16);
    }
    wait_vm<28>();
    bar();
    TS(16);
    unsigned ta[4];
#pragma unroll
    for (int c = 0; c < 4; ++c) ta[c] = kq * 9 * 4096 + trowP + ((c ^ frP) << 5);
    f32x4 acc[2][4];
#pragma unroll
    for (int t = 0; t < 2; ++t)
#pragma unroll
      for (int c = 0; c < 4; ++c) acc[t][c] = {0, 0, 0, 0};
#pragma unroll
    for (int i = 0; i < 14; ++i) {
      if (i == 5) {
        bar();
#pragma unroll
        for (int j = 9; j < 14; ++j) dma_wd(j);
      }
      if (i == 9) { wait_vm<0>(); bar(); }
      bf16x8 wf[4];
#pragma unroll
      for (int c = 0; c < 4; ++c) wf[c] = lds_tr2(ta[c] + (i % 9) * 4096, ta[c] + (i % 9) * 4096 + 2048);
#pragma unroll
      for (int t = 0; t < 2; ++t)
#pragma unroll
        for (int c = 0; c < 4; ++c) acc[t][c] = mfma32(wf[c], __builtin_bit_cast(bf16x8, af[i][t]), acc[t][c]);
    }
    TS(17);
    bar();
#pragma unroll
    for (int u = 0; u < 4; ++u) {
      if ((unsigned)u != kq) {
        const unsigned a = ((mh * 4 + u) * 4 + kq) * 2048 + lane * 16;
        *(LDSP f32x4*)(smem + a) = acc[u & 1][(u >> 1) * 2];
        *(LDSP f32x4*)(smem + a + 1024) = acc[u & 1][(u >> 1) * 2 + 1];
      }
    }
    f32x4 k0, k1;
    {
      const bool t1 = kq & 1, c1 = kq >> 1;
      const f32x4 a0 = t1 ? acc[1][0] : acc[0][0], a1 = t1 ? acc[1][1] : acc[0][1], a2 = t1 ? acc[1][2] : acc[0][2], a3 = t1 ? acc[1][3] : acc[0][3];
      k0 = c1 ? a2 : a0; k1 = c1 ? a3 : a1;
    }
    bar();
#pragma unroll
    for (int j = 1; j < 4; ++j) {
      const unsigned a = ((mh * 4 + kq) * 4 + (kq ^ j)) * 2048 + lane * 16;
      k0 += *(LDSP f32x4*)(smem + a);
      k1 += *(LDSP f32x4*)(smem + a + 1024);
    }
    const u32x4 av = swap_pair(pk(k0[0], k0[1]), pk(k0[2], k0[3]), pk(k1[0], k1[1]), pk(k1[2], k1[3]));
    u32x4 ov;
#pragma unroll
    for (int j = 0; j < 4; ++j) ov[j] = pk(blo(av[j]) + blo(hreg[j]), bhi(av[j]) + bhi(hreg[j]));
    __builtin_amdgcn_raw_buffer_store_b128(ov, mk(out, S_ * D_ * 2), (rt * 64 + (w & 3) * 16 + l15) * 1024 + cg * 128 + (w >> 2) * 64 + gp * 16, 0, 0);
    TS(18);
  }
}

template <int LO, int HI>
static void launch(void* const* d_in, void* d_out, void* d_ws, hipStream_t stream) {
  (void)hipFuncSetAttribute((const void*)fused_block<LO, HI>, hipFuncAttributeMaxDynamicSharedMemorySize, LDS_BYTES);
  fused_block<LO, HI><<<256, 512, LDS_BYTES, stream>>>((const u16*)d_in[0], (const u16*)d_in[1], (const u16*)d_in[2], (const u16*)d_in[4],
                                                       (const u16*)d_in[5], (const u16*)d_in[6], (u16*)d_out, (char*)d_ws);
}

extern "C" void kernel_launch(void* const* d_in, const int* in_sizes, int n_in, void* d_out, int out_size, void* d_ws,
                              size_t ws_size, hipStream_t stream) {
  launch<1, 4>(d_in, d_out, d_ws, stream);
}
```

```cpp
#include <hip/hip_runtime.h>
#include <hip/hip_bf16.h>

typedef unsigned short u16;
typedef unsigned long long u64;
typedef __attribute__((ext_vector_type(8))) __bf16 bf16x8;
typedef __attribute__((ext_vector_type(2))) __bf16 bf16x2;
typedef __attribute__((ext_vector_type(4))) float f32x4;
typedef __attribute__((ext_vector_type(2))) float f32x2;
typedef __attribute__((ext_vector_type(4))) short s16x4;
typedef __attribute__((ext_vector_type(8))) short s16x8;
typedef __attribute__((ext_vector_type(4))) unsigned u32x4;
typedef __attribute__((ext_vector_type(2))) unsigned u32x2;
#define LDSP __attribute__((address_space(3)))
#define GLB __attribute__((address_space(1)))
#define CST __attribute__((address_space(4)))
#define DI __device__ __forceinline__
#define SB() __builtin_amdgcn_sched_barrier(0)
typedef __amdgpu_buffer_rsrc_t rsrc_t;

constexpr int S_ = 2048, D_ = 512, F_ = 1792;
constexpr unsigned WS_EPOCH = 0;
constexpr unsigned WS_F1 = 4096;
constexpr unsigned WS_F2 = WS_F1 + 4096;
constexpr unsigned WS_F3 = WS_F2 + 4096;
constexpr unsigned WS_S1 = 16384;
constexpr unsigned WS_O = 65536;
constexpr unsigned WS_H = WS_O + 2097152;
constexpr unsigned WS_A = WS_H + 2097152;
constexpr unsigned WS_S2 = WS_A + 128 * 56 * 1024;
constexpr unsigned WS_END = WS_S2 + 128 * 2048;
constexpr unsigned SLOT = 34816;
constexpr unsigned L_SP = 4 * SLOT;
constexpr unsigned L_FS = L_SP;
constexpr unsigned L_CX = L_SP + 4096;
constexpr unsigned L_RED = L_SP + 4096;
constexpr unsigned L_CS3 = L_SP;
constexpr unsigned L_UP = 2 * SLOT;
constexpr unsigned L_GATE = 0;
constexpr unsigned LDS_BYTES = 163840;

extern __shared__ __attribute__((aligned(16))) char smem[];
#define TS(i)

DI unsigned rfl(unsigned v) { return __builtin_amdgcn_readfirstlane(v); }
DI rsrc_t mk(const void* p, unsigned bytes) { return __builtin_amdgcn_make_buffer_rsrc((void*)p, (short)0, (int)bytes, 0x00020000); }
DI void dma(unsigned lds, unsigned voff, rsrc_t r, unsigned soff) {
  asm volatile("s_mov_b32 m0, %0\n\ts_nop 0\n\tbuffer_load_dwordx4 %1, %2, %3 offen lds"
               :: "s"(lds + (unsigned)(size_t)(LDSP char*)smem), "v"(voff), "s"(r), "s"(soff) : "memory");
}
DI void dma_m(unsigned lds, unsigned voff, rsrc_t r, unsigned soff, u64 mask) {
  asm volatile("s_mov_b32 m0, %0\n\ts_mov_b64 exec, %4\n\tbuffer_load_dwordx4 %1, %2, %3 offen lds\n\ts_mov_b64 exec, -1"
               :: "s"(lds + (unsigned)(size_t)(LDSP char*)smem), "v"(voff), "s"(r), "s"(soff), "s"(mask) : "memory");
}
template <int OFF> DI void ld16c(u32x4& d, unsigned voff, rsrc_t r, unsigned soff) {
  asm volatile("buffer_load_dwordx4 %0, %1, %2, %3 offen offset:%4 sc1" : "=v"(d) : "v"(voff), "s"(r), "s"(soff), "n"(OFF) : "memory");
}
template <int OFF> DI void ld8c(u32x2& d, unsigned voff, rsrc_t r, unsigned soff) {
  asm volatile("buffer_load_dwordx2 %0, %1, %2, %3 offen offset:%4 sc1" : "=v"(d) : "v"(voff), "s"(r), "s"(soff), "n"(OFF) : "memory");
}
DI void ld16(u32x4& d, unsigned voff, rsrc_t r, unsigned soff) {
  asm volatile("buffer_load_dwordx4 %0, %1, %2, %3 offen" : "=v"(d) : "v"(voff), "s"(r), "s"(soff) : "memory");
}
template <int AUX> DI void st16(u32x4 v, rsrc_t r, unsigned voff, unsigned soff) {
  __builtin_amdgcn_raw_buffer_store_b128(v, r, voff, soff, AUX);
  asm volatile("s_nop 1" :: "v"(v) : "memory");
}
template <int N> DI void wait_vm() { asm volatile("s_waitcnt vmcnt(%0)" :: "n"(N) : "memory"); }
template <int N, class T> DI void vm_tie(T& a) { asm volatile("s_waitcnt vmcnt(%1)" : "+v"(a) : "n"(N) : "memory"); }
template <int N, class T> DI void vm_tie(T& a, T& b) { asm volatile("s_waitcnt vmcnt(%2)" : "+v"(a), "+v"(b) : "n"(N) : "memory"); }
template <class T> DI void tie(T& a) { asm volatile("" : "+v"(a)); }
DI void bar() { asm volatile("s_waitcnt lgkmcnt(0)\n\ts_barrier" ::: "memory"); }
DI bf16x8 lds_b128(unsigned a) { return *(LDSP bf16x8*)(smem + a); }
DI s16x4 lds_tr(unsigned a) { return __builtin_amdgcn_ds_read_tr16_b64_v4i16((LDSP s16x4*)(smem + a)); }
DI bf16x8 cat(s16x4 lo, s16x4 hi) { return __builtin_bit_cast(bf16x8, __builtin_shufflevector(lo, hi, 0, 1, 2, 3, 4, 5, 6, 7)); }
DI bf16x8 lds_tr2(unsigned a0, unsigned a1) { return cat(lds_tr(a0), lds_tr(a1)); }
DI bf16x8 asbf(u32x4 v) { return __builtin_bit_cast(bf16x8, v); }
DI f32x4 mfma32(bf16x8 a, bf16x8 b, f32x4 c) { return __builtin_amdgcn_mfma_f32_16x16x32_bf16(a, b, c, 0, 0, 0); }
DI f32x4 mfma16(u32x2 a, u32x2 b, f32x4 c) {
  return __builtin_amdgcn_mfma_f32_16x16x16bf16_1k(__builtin_bit_cast(s16x4, a), __builtin_bit_cast(s16x4, b), c, 0, 0, 0);
}
DI unsigned pk(float a, float b) { f32x2 f = {a, b}; return __builtin_bit_cast(unsigned, __builtin_convertvector(f, bf16x2)); }
DI float blo(unsigned v) { return __uint_as_float(v << 16); }
DI float bhi(unsigned v) { return __uint_as_float(v & 0xffff0000u); }
DI float dot2(unsigned a, unsigned b, float c) {
  return __builtin_amdgcn_fdot2_f32_bf16(__builtin_bit_cast(bf16x2, a), __builtin_bit_cast(bf16x2, b), c, false);
}
DI f32x2 swp16(float a, float b) {
  auto r = __builtin_amdgcn_permlane16_swap(__float_as_uint(a), __float_as_uint(b), false, false);
  f32x2 v = {__uint_as_float(r[0]), __uint_as_float(r[1])}; return v;
}
DI f32x2 swp32(float a, float b) {
  auto r = __builtin_amdgcn_permlane32_swap(__float_as_uint(a), __float_as_uint(b), false, false);
  f32x2 v = {__uint_as_float(r[0]), __uint_as_float(r[1])}; return v;
}
DI float sum2rows(float a, float b) { f32x2 p = swp16(a, b); float s = p[0] + p[1]; f32x2 q = swp32(s, s); return q[0] + q[1]; }
template <int CTRL> DI float dppf(float v) { return __int_as_float(__builtin_amdgcn_update_dpp(0, __float_as_int(v), CTRL, 0xf, 0xf, true)); }
DI u32x4 swap_pair(unsigned a0, unsigned a1, unsigned b0, unsigned b1) {
  auto r0 = __builtin_amdgcn_permlane16_swap(a0, b0, false, false);
  auto r1 = __builtin_amdgcn_permlane16_swap(a1, b1, false, false);
  u32x4 v = {r0[0], r1[0], r0[1], r1[1]};
  return v;
}
DI void publish(char* ws, unsigned off, u64 tag, int tid) {
  asm volatile("s_waitcnt vmcnt(0)" ::: "memory");
  bar();
  if (tid == 0) __hip_atomic_store((GLB u64*)(ws + off), tag, __ATOMIC_RELAXED, __HIP_MEMORY_SCOPE_AGENT);
}
DI void poll(char* ws, unsigned off, int n, u64 tag, int lane) {
  GLB u64* p = (GLB u64*)(ws + off) + (lane < n ? lane : 0);
#pragma unroll 1
  for (int spin = 0; spin < (1 << 20); ++spin) {
    u64 v = __hip_atomic_load(p, __ATOMIC_RELAXED, __HIP_MEMORY_SCOPE_AGENT);
    if (__all(v == tag)) break;
    __builtin_amdgcn_s_sleep(1);
  }
  asm volatile("" ::: "memory");
}

__global__ __launch_bounds__(512, 2) void fused_block(const u16* __restrict__ x, const u16* __restrict__ wqkv,
                                                      const u16* __restrict__ wo, const u16* __restrict__ wg,
                                                      const u16* __restrict__ wu, const u16* __restrict__ wd,
                                                      u16* __restrict__ out, char* __restrict__ ws) {
  const int tid = threadIdx.x, lane = tid & 63;
  const unsigned w = rfl(tid >> 6);
  const unsigned l15 = lane & 15, g = lane >> 4, q = (lane >> 2) & 3, p4 = lane & 3;
  const unsigned b = blockIdx.x, xcd = b & 7, bi = b >> 3;
  const rsrc_t rWS = mk(ws, WS_END);
  TS(0);
  const unsigned rt = xcd * 4 + (bi & 3), cg = bi >> 2;
  const unsigned panel = (xcd >> 1) * 2 + (bi & 1), fg = (xcd & 1) * 14 + (bi >> 1);
  const bool p3on = bi < 28;
  const unsigned prow = lane >> 3, pp = lane & 7;
  const unsigned fN = ((prow >> 1) & 1) | ((w & 1) << 1);
  const unsigned swzN = (((pp >> 1) ^ fN) << 5) | ((pp & 1) << 4);
  const unsigned frN = ((q >> 1) & 1) | ((g & 1) << 1);
  const unsigned trowN = (8 * g + q) * 128 + p4 * 8;
  const u32x4 ones = {0x3f803f80u, 0x3f803f80u, 0x3f803f80u, 0x3f803f80u};
  const rsrc_t rX = mk(x, S_ * D_ * 2);
  const unsigned vW3 = (8 * w + prow) * (F_ * 2) + (p3on ? fg : 0) * 128 + swzN;

  u64 tag, epoch;
  {
    const unsigned rg = (xcd >> 1) * 8 + (bi & 7), hg = (xcd & 1) * 4 + (bi >> 3);
    const unsigned rot = ((bi & 7) + 2 * (bi >> 3)) & 7;
    const rsrc_t rW = mk(wqkv, D_ * 1536 * 2), rO = mk(wo, D_ * D_ * 2), rU = mk(wu, D_ * F_ * 2);
    u32x4 xrow;
    ld16(xrow, lane * 16, rX, (rg * 64 + hg * 8 + w) * 1024);
    const int r0 = 8 * w + prow;
    const unsigned vX0 = max((int)rg * 64 - 16 + r0, 0) * 1024 + ((pp ^ ((r0 >> 1) & 7)) << 4);
    const unsigned vX1 = (rg * 64 + 48 + 2 * w + (prow & 1)) * 1024 + ((pp ^ w) << 4);
    const unsigned vW = (8 * w + prow) * 3072 + hg * 128 + swzN;
    const unsigned vO = (8 * w + prow) * 1024 + cg * 128 + swzN;
    const u64 mX = w == 0 ? ~0xffull : ~0ull;
    auto piece = [&](int G, int i) {
      if (G < 8) {
        const unsigned st = (G + rot) & 7, sb = (G & 3) * SLOT;
        if (i == 0) dma_m(sb + w * 1024, vX0, rX, st * 128, mX);
        else if (i == 1) dma_m(sb + 8192 + w * 256, vX1, rX, st * 128, 0xffffull);
        else dma(sb + 10240 + (i - 2) * 8192 + w * 1024, vW, rW, st * 64 * 3072 + (i - 2) * 1024);
      } else if (G < 10) {
        if (i < 4) dma((G - 8) * SLOT + (w + 8 * i) * 1024, vO, rO, (256 * (G - 8) + 64 * i) * 1024);
      } else {
        if (i < 4) dma(L_UP + (w + 8 * i) * 1024, vW3, rU, 64 * i * (F_ * 2));
      }
    };
#pragma unroll
    for (int G = 0; G < 4; ++G)
#pragma unroll
      for (int i = 0; i < 5; ++i) piece(G, i);
    if (tid < 32) *(LDSP u32x4*)(smem + (tid >> 3) * SLOT + (tid & 7) * 16) = ones;
    epoch = *(const CST u64*)(ws + WS_EPOCH);
    tag = epoch * 0x9E3779B97F4A7C15ull + 0x632BE59BD9B4E019ull;
    const unsigned tag32 = (unsigned)(tag >> 32);
    {
      vm_tie<20>(xrow);
      float s1 = 0.f, s2 = 0.f;
#pragma unroll
      for (int j = 0; j < 4; ++j) { s1 = dot2(xrow[j], 0x3f803f80u, s1); s2 = dot2(xrow[j], xrow[j], s2); }
      float s = sum2rows(s1, s2);
      s += dppf<0x128>(s); s += dppf<0x124>(s); s += dppf<0x122>(s); s += dppf<0x121>(s);
      const float m = __int_as_float(__builtin_amdgcn_readlane(__float_as_int(s), 0)) * (1.f / 512.f);
      const float e2 = __int_as_float(__builtin_amdgcn_readlane(__float_as_int(s), 16)) * (1.f / 512.f);
      const float rs = __builtin_amdgcn_rsqf(fmaxf(e2 - m * m, 0.f) + 1e-5f);
      const u32x4 gr = {__float_as_uint(m), tag32, __float_as_uint(rs), tag32};
      st16<16>(gr, rWS, lane == 0 ? 0u : 0x7ff00000u, WS_S1 + (rg * 64 + hg * 8 + w) * 16);
    }
    const unsigned hw = w & 3, half = w >> 2;
    const unsigned xa = (l15 * 128 + ((g ^ (l15 >> 1)) << 4) + half * 4096) ^ (half * 64);
    const unsigned wa = (10240 + trowN + ((hw ^ frN) << 5)) + half * 4096;
    const unsigned xb = xa ^ 64, wb = wa + 4096 - half * 8192;
    const unsigned par = l15 & 1;
    f32x4 mc, mp, mp1;
#pragma unroll
    for (int r = 0; r < 4; ++r) {
      const bool pm = (r & 1) == par, cur = 4 * g + r <= l15;
      mc[r] = (pm && cur) ? 0.f : -4e9f;
      mp[r] = (pm && !cur) ? 0.f : -4e9f;
      mp1[r] = (rg == 0 && half == 0) ? -4e9f : mp[r];
    }
    const unsigned se0 = par ? 0x01000c0cu : 0x0c0c0100u, se1 = par ? 0x03020c0cu : 0x0c0c0302u;
    const bool c0 = (4 * g + par) <= l15, c1 = (4 * g + 2 + par) <= l15;
    u32x4 sel = {c0 ? 0x0c0c0c0cu : se0, c1 ? 0x0c0c0c0cu : se1, c0 ? se0 : 0x0c0c0c0cu, c1 ? se1 : 0x0c0c0c0cu};
    unsigned vS = WS_S1 + max((int)(rg * 64 - 16 + half * 32 + (g < 3 ? g : 0) * 16 + l15), 0) * 16;
    tie(mc); tie(mp); tie(mp1); tie(sel); tie(vS);

    f32x4 aQ[2], aK[3], aV[3], aC = {0, 0, 0, 0};
#pragma unroll
    for (int i = 0; i < 3; ++i) { aK[i] = {0, 0, 0, 0}; aV[i] = {0, 0, 0, 0}; }
    aQ[0] = {0, 0, 0, 0}; aQ[1] = {0, 0, 0, 0};
    bf16x8 xf[2][3];
    s16x4 wl[2][3], wh[2][3];
    auto rdx = [&](int j, int t) { xf[j & 1][t] = lds_b128(((j >> 1) & 3) * SLOT + ((j & 1) ? xb : xa) + t * 2048); };
    auto rdw = [&](int j, int m) { const unsigned a = ((j >> 1) & 3) * SLOT + ((j & 1) ? wb : wa) + m * 8192;
                                   wl[j & 1][m] = lds_tr(a); wh[j & 1][m] = lds_tr(a + 512); };
    wait_vm<16>();
    bar();
    TS(1);
    rdx(0, 0); rdw(0, 1); rdw(0, 2); rdx(0, 1); rdx(0, 2); rdw(0, 0);
    u32x4 sl;
    SB();
#pragma unroll
    for (int j = 0; j < 16; ++j) {
      const int c = j & 1, G = (j + 7) >> 1, i0 = c ? 0 : 3;
      const bool nx = j < 15, dm = j >= 1 && j < 15;
      if (c && nx) {
        if (j <= 5) wait_vm<11>(); else if (j <= 9) wait_vm<10>(); else if (j == 11) wait_vm<10>(); else vm_tie<8>(sl);
        bar();
        if (j == 7) TS(2);
        if (j == 9) ld16c<0>(sl, vS, rWS, 0);
      }
      const bf16x8 wq = cat(wl[c][0], wh[c][0]), wk = cat(wl[c][1], wh[c][1]), wv = cat(wl[c][2], wh[c][2]);
      if (nx) { rdx(j + 1, 0); rdw(j + 1, 1); }
      SB(); aK[0] = mfma32(wk, xf[c][0], aK[0]); SB();
      if (nx) rdw(j + 1, 2);
      SB(); aV[0] = mfma32(xf[c][0], wv, aV[0]); SB();
      if (nx) { rdx(j + 1, 1); rdx(j + 1, 2); }
      SB(); aK[1] = mfma32(wk, xf[c][1], aK[1]); SB();
      if (nx) rdw(j + 1, 0);
      SB(); aV[1] = mfma32(xf[c][1], wv, aV[1]); SB();
      if (dm) piece(G, i0);
      if (j == 13) {
#pragma unroll 1
        for (int spin = 0; spin < (1 << 20); ++spin) {
          if (__all(sl[1] == tag32 && sl[3] == tag32)) break;
          __builtin_amdgcn_s_sleep(1);
          ld16c<0>(sl, vS, rWS, 0);
          vm_tie<0>(sl);
        }
        *(LDSP unsigned*)(smem + L_FS + w * 512 + lane * 4) = sl[0];
        *(LDSP unsigned*)(smem + L_FS + w * 512 + 256 + lane * 4) = sl[2];
      }
      SB(); aK[2] = mfma32(wk, xf[c][2], aK[2]); aV[2] = mfma32(xf[c][2], wv, aV[2]); SB();
      if (dm) piece(G, i0 + 1);
      SB(); aQ[0] = mfma32(wq, xf[c][1], aQ[0]); aQ[1] = mfma32(wq, xf[c][2], aQ[1]); SB();
      if (dm && c) piece(G, 2);
      if (!c) aC = mfma32(wq, asbf(ones), aC);
      SB();
    }
    TS(3);
    *(LDSP f32x4*)(smem + L_CX + w * 2304 + lane * 16) = aC;
    *(LDSP f32x4*)(smem + L_CX + w * 2304 + 1024 + lane * 16) = aK[0];
    *(LDSP float*)(smem + L_CX + w * 2304 + 2048 + lane * 4) = aV[0][0];
    float sm[3], sr[3];
    f32x4 vm[3], vr[3];
#pragma unroll
    for (int t = 0; t < 3; ++t) {
      sm[t] = *(LDSP float*)(smem + L_FS + w * 512 + t * 64 + l15 * 4);
      sr[t] = *(LDSP float*)(smem + L_FS + w * 512 + 256 + t * 64 + l15 * 4);
      vm[t] = *(LDSP f32x4*)(smem + L_FS + w * 512 + t * 64 + g * 16);
      vr[t] = *(LDSP f32x4*)(smem + L_FS + w * 512 + 256 + t * 64 + g * 16);
    }
    bar();
    const f32x4 csq = aC + *(LDSP f32x4*)(smem + L_CX + (w ^ 4) * 2304 + lane * 16);
    const f32x4 csk = *(LDSP f32x4*)(smem + L_CX + hw * 2304 + 1024 + g * 256);
    const float csv = *(LDSP float*)(smem + L_CX + hw * 2304 + 2048 + l15 * 4);
    u32x2 qb[2], kb[3], vb[3];
#pragma unroll
    for (int t = 0; t < 3; ++t) {
      const f32x4 kv = (aK[t] - sm[t] * csk) * sr[t];
      kb[t] = {pk(kv[0], kv[1]), pk(kv[2], kv[3])};
      if (t > 0) { const f32x4 qv = (aQ[t - 1] - sm[t] * csq) * sr[t]; qb[t - 1] = {pk(qv[0], qv[1]), pk(qv[2], qv[3])}; }
      const f32x4 vv = (aV[t] - vm[t] * csv) * vr[t];
      vb[t] = {pk(vv[0], vv[1]), pk(vv[2], vv[3])};
    }
    float l0[2], l1[2];
#pragma unroll
    for (int t = 0; t < 2; ++t) {
      const f32x4 sc = mfma16(kb[t + 1], qb[t], mc), sp = mfma16(kb[t], qb[t], t ? mp : mp1);
      l0[t] = fmaxf(fmaxf(fmaxf(sc[0], sc[1]), sp[0]), sp[1]);
      l1[t] = fmaxf(fmaxf(fmaxf(sc[2], sc[3]), sp[2]), sp[3]);
    }
    f32x2 mx;
    {
      const f32x2 a = swp16(fmaxf(l0[0], l1[0]), fmaxf(l0[1], l1[1]));
      const float m1 = fmaxf(a[0], a[1]);
      const f32x2 c = swp32(m1, m1);
      const float m2 = fmaxf(c[0], c[1]) * (-0.25f * 1.44269504f);
      mx = swp16(m2, m2);
    }
    unsigned ob[2][2];
#pragma unroll
    for (int t = 0; t < 2; ++t) {
      const float e0 = __builtin_amdgcn_exp2f(fmaf(l0[t], 0.25f * 1.44269504f, mx[t])), e1 = __builtin_amdgcn_exp2f(fmaf(l1[t], 0.25f * 1.44269504f, mx[t]));
      const unsigned e = pk(e0, e1);
      u32x4 pf;
#pragma unroll
      for (int j = 0; j < 4; ++j) pf[j] = __builtin_amdgcn_perm(0u, e, sel[j]);
      const u32x4 vf = {vb[t][0], vb[t][1], vb[t + 1][0], vb[t + 1][1]};
      const f32x4 z = {0, 0, 0, 0};
      const f32x4 den = mfma32(asbf(ones), asbf(pf), z);
      const f32x4 o = mfma32(asbf(vf), asbf(pf), z) * __builtin_amdgcn_rcpf(den[0]);
      ob[t][0] = pk(o[0], o[1]); ob[t][1] = pk(o[2], o[3]);
    }
    {
      const u32x4 ov = swap_pair(ob[0][0], ob[0][1], ob[1][0], ob[1][1]);
      const unsigned head = hg * 4 + hw;
      const unsigned off = (g & 1) * 16384 + ((g >> 1) * 16 + l15) * 16;
      st16<16>(ov, rWS, off, WS_O + ((rg * 4 + half * 2) * 16 + (head >> 1)) * 1024 + (head & 1) * 512);
    }
    TS(4);
    publish(ws, WS_F1 + (rg * 16 + hg) * 8, tag, tid);
    TS(5);
#pragma unroll
    for (int i = 4; i < 8; ++i) dma(L_UP + (w + 8 * i) * 1024, vW3, rU, 64 * i * (F_ * 2));
  }

  u32x4 hreg;
  const unsigned gp = ((g & 1) << 1) | (g >> 1);
  {
    const unsigned m = w & 3, kh = w >> 2;
    u32x4 xr;
    ld16(xr, (m * 16 + l15) * 1024 + gp * 16, rX, rt * 65536 + cg * 128 + kh * 64);
    bf16x8 wf[8][4];
#pragma unroll
    for (int ks = 0; ks < 8; ++ks)
#pragma unroll
      for (int c = 0; c < 4; ++c) {
        const unsigned a = kh * SLOT + trowN + ((c ^ (2 * kh) ^ frN) << 5) + ks * 4096;
        wf[ks][c] = lds_tr2(a, a + 512);
      }
    bar();
    {
      const rsrc_t rG = mk(wg, D_ * F_ * 2);
#pragma unroll
      for (int i = 0; i < 8; ++i) dma(L_GATE + (w + 8 * i) * 1024, vW3, rG, 64 * i * (F_ * 2));
    }
    TS(6);
    poll(ws, WS_F1 + (rt * 16 + kh * 4) * 8, 4, tag, lane);
    TS(7);
    u32x4 of[8];
    const unsigned ob = WS_O + ((rt * 4 + m) * 16 + kh * 8) * 1024;
    ld16c<0>(of[0], lane * 16, rWS, ob); ld16c<1024>(of[1], lane * 16, rWS, ob); ld16c<2048>(of[2], lane * 16, rWS, ob); ld16c<3072>(of[3], lane * 16, rWS, ob);
    ld16c<0>(of[4], lane * 16, rWS, ob + 4096); ld16c<1024>(of[5], lane * 16, rWS, ob + 4096); ld16c<2048>(of[6], lane * 16, rWS, ob + 4096); ld16c<3072>(of[7], lane * 16, rWS, ob + 4096);
    f32x4 acc[4];
#pragma unroll
    for (int c = 0; c < 4; ++c) acc[c] = {0, 0, 0, 0};
    auto step = [&](int ks) {
#pragma unroll
      for (int c = 3; c >= 0; --c) acc[c] = mfma32(wf[ks][c], asbf(of[ks]), acc[c]);
      SB();
    };
    vm_tie<7>(of[0], xr); step(0); vm_tie<6>(of[1]); step(1); vm_tie<5>(of[2]); step(2); vm_tie<4>(of[3]); step(3);
    vm_tie<3>(of[4]); step(4); vm_tie<2>(of[5]); step(5); vm_tie<1>(of[6]); step(6); vm_tie<0>(of[7]); step(7);
    TS(8);
    *(LDSP f32x4*)(smem + L_RED + w * 2048 + lane * 16) = acc[2];
    *(LDSP f32x4*)(smem + L_RED + w * 2048 + 1024 + lane * 16) = acc[3];
    bar();
    const f32x4 k0 = acc[0] + *(LDSP f32x4*)(smem + L_RED + (w ^ 4) * 2048 + lane * 16);
    const f32x4 k1 = acc[1] + *(LDSP f32x4*)(smem + L_RED + (w ^ 4) * 2048 + 1024 + lane * 16);
    const u32x4 av = swap_pair(pk(k0[0], k0[1]), pk(k0[2], k0[3]), pk(k1[0], k1[1]), pk(k1[2], k1[3]));
#pragma unroll
    for (int j = 0; j < 4; ++j) hreg[j] = pk(blo(av[j]) + blo(xr[j]), bhi(av[j]) + bhi(xr[j]));
    st16<16>(hreg, rWS, (gp * 16 + l15) * 16, WS_H + ((rt * 4 + m) * 16 + cg * 2 + kh) * 1024);
    float s1 = 0.f, s2 = 0.f;
#pragma unroll
    for (int j = 0; j < 4; ++j) { s1 = dot2(hreg[j], 0x3f803f80u, s1); s2 = dot2(hreg[j], hreg[j], s2); }
    const float ss = sum2rows(s1, s2);
    __builtin_amdgcn_raw_buffer_store_b32(__float_as_uint(ss), rWS, g < 2 ? l15 * 8 + g * 4 : 0x7ff00000u, WS_S2 + ((rt * 4 + m) * 16 + cg * 2 + kh) * 128, 16);
    publish(ws, WS_F2 + (rt * 16 + cg) * 8, tag, tid);
    TS(9);
  }

  if (p3on) {
    {
      const unsigned a = (w >> 2) * L_UP + trowN + (((w & 3) ^ frN) << 5);
      f32x4 cs = {0, 0, 0, 0};
#pragma unroll
      for (int ks = 0; ks < 16; ++ks) cs = mfma32(lds_tr2(a + ks * 4096, a + ks * 4096 + 512), asbf(ones), cs);
      if (l15 == 0) *(LDSP f32x4*)(smem + L_CS3 + w * 64 + g * 16) = cs;
      bar();
    }
    const unsigned rot = (bi >> 1) & 15;
    unsigned ta[4];
#pragma unroll
    for (int c = 0; c < 4; ++c) ta[c] = trowN + ((c ^ frN) << 5);
    s16x4 fl[2][8], fh[2][8];
    auto rdw = [&](int kk, int i) {
      const unsigned a = ((kk + rot) & 15) * 4096 + ta[i >> 1] + (i & 1) * L_UP;
      fl[kk & 1][i] = lds_tr(a); fh[kk & 1][i] = lds_tr(a + 512);
    };
#pragma unroll
    for (int i = 0; i < 8; ++i) rdw(0, i);
    TS(10);
    poll(ws, WS_F2 + ((panel * 4 + (w >> 1)) * 16) * 8, 8, tag, lane);
    TS(11);
    const unsigned hb = WS_H + (panel * 16 + 2 * w) * 16 * 1024;
    f32x4 acc[2][8];
#pragma unroll
    for (int t = 0; t < 2; ++t)
#pragma unroll
      for (int i = 0; i < 8; ++i) acc[t][i] = {0, 0, 0, 0};
    constexpr int PF = 4;
    u32x4 hf[PF + 1][2];
    u32x2 sp[2][4];
    const unsigned vh0 = lane * 16, vh1 = lane * 16 + 16384;
    auto ldh = [&](int kk) {
      const unsigned so = hb + ((kk + rot) & 15) * 1024;
      ld16c<0>(hf[kk % (PF + 1)][0], vh0, rWS, so);
      ld16c<0>(hf[kk % (PF + 1)][1], vh1, rWS, so);
    };
#pragma unroll
    for (int kk = 0; kk < PF; ++kk) ldh(kk);
    SB();
#pragma unroll
    for (int kk = 0; kk < 16; ++kk) {
      const int c = kk & 1, hs = kk % (PF + 1);
      if (kk + PF < 16) ldh(kk + PF);
      else {
        const int u = kk + PF - 16;
        const unsigned so = WS_S2 + (panel * 16 + 2 * w + (u >> 1)) * 2048, vs = g * 512 + l15 * 8;
        if (u & 1) { ld8c<256>(sp[u >> 1][2], vs, rWS, so); ld8c<384>(sp[u >> 1][3], vs, rWS, so); }
        else { ld8c<0>(sp[u >> 1][0], vs, rWS, so); ld8c<128>(sp[u >> 1][1], vs, rWS, so); }
      }
      vm_tie<2 * PF>(hf[hs][0], hf[hs][1]);
#pragma unroll
      for (int i = 0; i < 8; ++i) {
        const bf16x8 wf = cat(fl[c][i], fh[c][i]);
        SB(); acc[0][i] = mfma32(wf, asbf(hf[hs][0]), acc[0][i]); SB();
        if (kk < 15) rdw(kk + 1, i);
        SB(); acc[1][i] = mfma32(wf, asbf(hf[hs][1]), acc[1][i]); SB();
      }
    }
    TS(12);
    f32x4 cs[8];
#pragma unroll
    for (int i = 0; i < 8; ++i) cs[i] = *(LDSP f32x4*)(smem + L_CS3 + ((i & 1) * 4 + (i >> 1)) * 64 + g * 16);
    asm volatile("s_waitcnt vmcnt(0)" : "+v"(sp[0][0]), "+v"(sp[0][1]), "+v"(sp[0][2]), "+v"(sp[0][3]), "+v"(sp[1][0]), "+v"(sp[1][1]), "+v"(sp[1][2]), "+v"(sp[1][3]) :: "memory");
#pragma unroll
    for (int t = 0; t < 2; ++t) {
      const f32x2 s = (__builtin_bit_cast(f32x2, sp[t][0]) + __builtin_bit_cast(f32x2, sp[t][1])) + (__builtin_bit_cast(f32x2, sp[t][2]) + __builtin_bit_cast(f32x2, sp[t][3]));
      const float r = sum2rows(s[0], s[1]) * (1.f / 512.f);
      const f32x2 me = swp16(r, r);
      const float var = fmaxf(me[1] - me[0] * me[0], 0.f) + 1e-5f;
      const float ke = __builtin_amdgcn_rsqf(var) * -1.44269504f;
      unsigned av[4][2];
#pragma unroll
      for (int c = 0; c < 4; ++c) {
        const f32x4 gg = acc[t][2 * c] - me[0] * cs[2 * c], uu = acc[t][2 * c + 1] - me[0] * cs[2 * c + 1];
        const f32x4 ar = gg * ke;
        f32x4 e;
#pragma unroll
        for (int j = 0; j < 4; ++j) e[j] = __builtin_amdgcn_exp2f(ar[j]);
        const f32x4 dn = e * var + var;
        f32x4 rc;
#pragma unroll
        for (int j = 0; j < 4; ++j) rc[j] = __builtin_amdgcn_rcpf(dn[j]);
        const f32x4 r4 = (gg * uu) * rc;
        av[c][0] = pk(r4[0], r4[1]); av[c][1] = pk(r4[2], r4[3]);
      }
#pragma unroll
      for (int cp = 0; cp < 2; ++cp) {
        const u32x4 v = {av[2 * cp][0], av[2 * cp][1], av[2 * cp + 1][0], av[2 * cp + 1][1]};
        st16<16>(v, rWS, lane * 16, WS_A + ((panel * 16 + 2 * w + t) * 56 + fg * 2 + cp) * 1024);
      }
    }
    TS(13);
    publish(ws, WS_F3 + (panel * 32 + fg) * 8, tag, tid);
    TS(14);
  } else if (b == 255 && w == 0) {
#pragma unroll 1
    for (int j = 0; j < 4; ++j) {
      GLB u64* p = (GLB u64*)(ws + WS_F1) + (j * 8 + (lane >> 3)) * 16 + (lane & 7);
#pragma unroll 1
      for (int spin = 0; spin < (1 << 20); ++spin) {
        u64 v = __hip_atomic_load(p, __ATOMIC_RELAXED, __HIP_MEMORY_SCOPE_AGENT);
        if (__all(v == tag)) break;
        __builtin_amdgcn_s_sleep(1);
      }
    }
    if (lane == 0) __hip_atomic_store((GLB u64*)(ws + WS_EPOCH), epoch + 1, __ATOMIC_RELAXED, __HIP_MEMORY_SCOPE_AGENT);
  }

  {
    const unsigned mh = (w >> 1) & 1, kq = (w & 1) + 2 * (w >> 2);
    const unsigned rot = (cg * 7) >> 2;
    const rsrc_t rD = mk(wd, D_ * F_ * 2);
    const unsigned swzP = (((pp >> 1) ^ ((prow >> 1) & 3)) << 5) | ((pp & 1) << 4);
    const unsigned vD = (8 * (w & 3) + prow) * 1024 + cg * 128 + swzP;
    auto rk = [&](int i) { const unsigned s = i + rot; return s >= 14 ? s - 14 : s; };
    auto dma_wd = [&](int i, int j) {
      const unsigned k = (w >> 2) + 2 * j;
      dma((k * 9 + (i % 9)) * 4096 + (w & 3) * 1024, vD, rD, (k * 14 + rk(i)) * 32 * 1024);
    };
#pragma unroll
    for (int i = 0; i < 9; ++i) { dma_wd(i, 0); dma_wd(i, 1); }
    poll(ws, WS_F3 + ((rt >> 2) * 32 + kq * 7) * 8, 7, tag, lane);
    TS(15);
    constexpr int PF = 5;
    u32x4 af[PF + 1][2];
    const unsigned ab = WS_A + ((rt * 4 + 2 * mh) * 56 + kq * 14) * 1024;
    const unsigned va0 = lane * 16 + (kq & 1) * 56 * 1024, va1 = lane * 16 + ((kq & 1) ^ 1) * 56 * 1024;
    auto lda = [&](int i) {
      const unsigned so = ab + rk(i) * 1024;
      ld16c<0>(af[i % (PF + 1)][0], va0, rWS, so);
      ld16c<0>(af[i % (PF + 1)][1], va1, rWS, so);
    };
#pragma unroll
    for (int i = 0; i < PF; ++i) lda(i);
    bar();
    TS(16);
    const unsigned frP = ((4 * g + q) >> 1) & 3;
    const unsigned trowP = (4 * g + q) * 128 + p4 * 8;
    unsigned ta[4];
#pragma unroll
    for (int c = 0; c < 4; ++c) ta[c] = kq * 9 * 4096 + trowP + ((c ^ (kq & 2) ^ frP) << 5);
    f32x4 acc[2][4];
#pragma unroll
    for (int t = 0; t < 2; ++t)
#pragma unroll
      for (int c = 0; c < 4; ++c) acc[t][c] = {0, 0, 0, 0};
    s16x4 fl[2][4], fh[2][4];
    auto rdw = [&](int i, int c) { const unsigned a = ta[c] + (i % 9) * 4096; fl[i & 1][c] = lds_tr(a); fh[i & 1][c] = lds_tr(a + 2048); };
#pragma unroll
    for (int c = 0; c < 4; ++c) rdw(0, c);
    SB();
#pragma unroll
    for (int i = 0; i < 14; ++i) {
      const int cc = i & 1, as = i % (PF + 1);
      if (i == 4) {
        bar();
#pragma unroll
        for (int j = 9; j < 14; ++j) { dma_wd(j, 0); dma_wd(j, 1); }
      }
      if (i + PF < 14) lda(i + PF);
      if (i < 4) vm_tie<10>(af[as][0], af[as][1]);
      else if (i < 8) vm_tie<20>(af[as][0], af[as][1]);
      else if (i == 8) { vm_tie<10>(af[as][0], af[as][1]); bar(); }
      else if (i == 9) vm_tie<8>(af[as][0], af[as][1]);
      else if (i == 10) vm_tie<6>(af[as][0], af[as][1]);
      else if (i == 11) vm_tie<4>(af[as][0], af[as][1]);
      else if (i == 12) vm_tie<2>(af[as][0], af[as][1]);
      else vm_tie<0>(af[as][0], af[as][1]);
#pragma unroll
      for (int c = 3; c >= 0; --c) {
        const bf16x8 wf = cat(fl[cc][c], fh[cc][c]);
        SB(); acc[1][c] = mfma32(wf, asbf(af[as][1]), acc[1][c]); SB();
        if (i < 13) rdw(i + 1, c);
        SB(); acc[0][c] = mfma32(wf, asbf(af[as][0]), acc[0][c]); SB();
      }
    }
    TS(17);
    bar();
#pragma unroll
    for (int u = 3; u >= 1; --u) {
      const unsigned a = ((mh * 4 + (u ^ kq)) * 4 + kq) * 2048 + lane * 16;
      *(LDSP f32x4*)(smem + a) = acc[u & 1][(u >> 1) * 2];
      *(LDSP f32x4*)(smem + a + 1024) = acc[u & 1][(u >> 1) * 2 + 1];
    }
    f32x4 k0 = acc[0][0], k1 = acc[0][1];
    bar();
#pragma unroll
    for (int j = 1; j < 4; ++j) {
      const unsigned a = ((mh * 4 + kq) * 4 + (kq ^ j)) * 2048 + lane * 16;
      k0 += *(LDSP f32x4*)(smem + a);
      k1 += *(LDSP f32x4*)(smem + a + 1024);
    }
    const u32x4 av = swap_pair(pk(k0[0], k0[1]), pk(k0[2], k0[3]), pk(k1[0], k1[1]), pk(k1[2], k1[3]));
    u32x4 ov;
#pragma unroll
    for (int j = 0; j < 4; ++j) ov[j] = pk(blo(av[j]) + blo(hreg[j]), bhi(av[j]) + bhi(hreg[j]));
    st16<0>(ov, mk(out, S_ * D_ * 2), l15 * 1024 + gp * 16, (rt * 64 + (w & 3) * 16) * 1024 + cg * 128 + (w >> 2) * 64);
    TS(18);
  }
}

extern "C" void kernel_launch(void* const* d_in, const int* in_sizes, int n_in, void* d_out, int out_size, void* d_ws,
                              size_t ws_size, hipStream_t stream) {
  (void)hipFuncSetAttribute((const void*)fused_block, hipFuncAttributeMaxDynamicSharedMemorySize, LDS_BYTES);
  fused_block<<<256, 512, LDS_BYTES, stream>>>((const u16*)d_in[0], (const u16*)d_in[1], (const u16*)d_in[2], (const u16*)d_in[4],
                                               (const u16*)d_in[5], (const u16*)d_in[6], (u16*)d_out, (char*)d_ws);
}
```

```cpp
#include <hip/hip_runtime.h>
#include <hip/hip_bf16.h>

typedef unsigned short u16;
typedef unsigned long long u64;
typedef __attribute__((ext_vector_type(8))) __bf16 bf16x8;
typedef __attribute__((ext_vector_type(2))) __bf16 bf16x2;
typedef __attribute__((ext_vector_type(4))) float f32x4;
typedef __attribute__((ext_vector_type(2))) float f32x2;
typedef __attribute__((ext_vector_type(4))) short s16x4;
typedef __attribute__((ext_vector_type(8))) short s16x8;
typedef __attribute__((ext_vector_type(4))) unsigned u32x4;
typedef __attribute__((ext_vector_type(2))) unsigned u32x2;
#define LDSP __attribute__((address_space(3)))
#define GLB __attribute__((address_space(1)))
#define CST __attribute__((address_space(4)))
#define DI __device__ __forceinline__
#define SB() __builtin_amdgcn_sched_barrier(0)
typedef __amdgpu_buffer_rsrc_t rsrc_t;

constexpr int S_ = 2048, D_ = 512, F_ = 1792;
constexpr unsigned WS_EPOCH = 0;
constexpr unsigned WS_F1 = 4096;
constexpr unsigned WS_F2 = WS_F1 + 4096;
constexpr unsigned WS_S1 = 16384;
constexpr unsigned WS_F3 = 65536;
constexpr unsigned WS_O = 131072;
constexpr unsigned WS_H = WS_O + 2097152;
constexpr unsigned WS_A = WS_H + 2097152;
constexpr unsigned WS_S2 = WS_A + 128 * 56 * 1024;
constexpr unsigned WS_END = WS_S2 + 128 * 2048;
constexpr unsigned SLOT = 34816;
constexpr unsigned L_SP = 4 * SLOT;
constexpr unsigned L_FS = L_SP;
constexpr unsigned L_CX = L_SP + 4096;
constexpr unsigned L_RED = L_SP + 4096;
constexpr unsigned L_ST = 39 * 4096;
constexpr unsigned L_UP = 65536;
constexpr unsigned L_GATE = 0;
constexpr unsigned LDS_BYTES = 163840;

extern __shared__ __attribute__((aligned(16))) char smem[];
#define TS(i)
#define TS3(i)
#define PRIO(x) do { SB(); __builtin_amdgcn_s_setprio(x); SB(); } while (0)

DI unsigned rfl(unsigned v) { return __builtin_amdgcn_readfirstlane(v); }
DI rsrc_t mk(const void* p, unsigned bytes) { return __builtin_amdgcn_make_buffer_rsrc((void*)p, (short)0, (int)bytes, 0x00020000); }
DI void dma(unsigned lds, unsigned voff, rsrc_t r, unsigned soff) {
  asm volatile("s_mov_b32 m0, %0\n\ts_nop 0\n\tbuffer_load_dwordx4 %1, %2, %3 offen lds"
               :: "s"(lds + (unsigned)(size_t)(LDSP char*)smem), "v"(voff), "s"(r), "s"(soff) : "memory");
}
DI void dma_m(unsigned lds, unsigned voff, rsrc_t r, unsigned soff, u64 mask) {
  asm volatile("s_mov_b32 m0, %0\n\ts_mov_b64 exec, %4\n\tbuffer_load_dwordx4 %1, %2, %3 offen lds\n\ts_mov_b64 exec, -1"
               :: "s"(lds + (unsigned)(size_t)(LDSP char*)smem), "v"(voff), "s"(r), "s"(soff), "s"(mask) : "memory");
}
DI void dmac(unsigned lds, unsigned voff, rsrc_t r, unsigned soff) {
  asm volatile("s_mov_b32 m0, %0\n\ts_nop 0\n\tbuffer_load_dwordx4 %1, %2, %3 offen sc1 lds"
               :: "s"(lds + (unsigned)(size_t)(LDSP char*)smem), "v"(voff), "s"(r), "s"(soff) : "memory");
}
template <int OFF> DI void ld16c(u32x4& d, unsigned voff, rsrc_t r, unsigned soff) {
  asm volatile("buffer_load_dwordx4 %0, %1, %2, %3 offen offset:%4 sc1" : "=v"(d) : "v"(voff), "s"(r), "s"(soff), "n"(OFF) : "memory");
}
template <int OFF> DI void ld8c(u32x2& d, unsigned voff, rsrc_t r, unsigned soff) {
  asm volatile("buffer_load_dwordx2 %0, %1, %2, %3 offen offset:%4 sc1" : "=v"(d) : "v"(voff), "s"(r), "s"(soff), "n"(OFF) : "memory");
}
DI void ld4c(unsigned& d, unsigned voff, rsrc_t r, unsigned soff) {
  asm volatile("buffer_load_dword %0, %1, %2, %3 offen sc1" : "=v"(d) : "v"(voff), "s"(r), "s"(soff) : "memory");
}
DI void ld16(u32x4& d, unsigned voff, rsrc_t r, unsigned soff) {
  asm volatile("buffer_load_dwordx4 %0, %1, %2, %3 offen" : "=v"(d) : "v"(voff), "s"(r), "s"(soff) : "memory");
}
template <int AUX> DI void st16(u32x4 v, rsrc_t r, unsigned voff, unsigned soff) {
  __builtin_amdgcn_raw_buffer_store_b128(v, r, voff, soff, AUX);
  asm volatile("s_nop 1" :: "v"(v) : "memory");
}
template <int N> DI void wait_vm() { asm volatile("s_waitcnt vmcnt(%0)" :: "n"(N) : "memory"); }
template <int N, class T> DI void vm_tie(T& a) { asm volatile("s_waitcnt vmcnt(%1)" : "+v"(a) : "n"(N) : "memory"); }
template <int N, class T> DI void vm_tie(T& a, T& b) { asm volatile("s_waitcnt vmcnt(%2)" : "+v"(a), "+v"(b) : "n"(N) : "memory"); }
template <class T> DI void tie(T& a) { asm volatile("" : "+v"(a)); }
DI void wait_lds() { asm volatile("" ::: "memory"); __builtin_amdgcn_s_waitcnt(0xC07F); asm volatile("" ::: "memory"); }
DI void bar() { wait_lds(); asm volatile("s_barrier" ::: "memory"); }
DI bf16x8 lds_b128(unsigned a) { return *(LDSP bf16x8*)(smem + a); }
DI s16x4 lds_tr(unsigned a) { return __builtin_amdgcn_ds_read_tr16_b64_v4i16((LDSP s16x4*)(smem + a)); }
DI bf16x8 cat(s16x4 lo, s16x4 hi) { return __builtin_bit_cast(bf16x8, __builtin_shufflevector(lo, hi, 0, 1, 2, 3, 4, 5, 6, 7)); }
DI bf16x8 lds_tr2(unsigned a0, unsigned a1) { return cat(lds_tr(a0), lds_tr(a1)); }
DI bf16x8 asbf(u32x4 v) { return __builtin_bit_cast(bf16x8, v); }
DI f32x4 mfma32(bf16x8 a, bf16x8 b, f32x4 c) { return __builtin_amdgcn_mfma_f32_16x16x32_bf16(a, b, c, 0, 0, 0); }
DI f32x4 mfma16(u32x2 a, u32x2 b, f32x4 c) {
  return __builtin_amdgcn_mfma_f32_16x16x16bf16_1k(__builtin_bit_cast(s16x4, a), __builtin_bit_cast(s16x4, b), c, 0, 0, 0);
}
DI unsigned pk(float a, float b) { f32x2 f = {a, b}; return __builtin_bit_cast(unsigned, __builtin_convertvector(f, bf16x2)); }
DI float blo(unsigned v) { return __uint_as_float(v << 16); }
DI float bhi(unsigned v) { return __uint_as_float(v & 0xffff0000u); }
DI float dot2(unsigned a, unsigned b, float c) {
  return __builtin_amdgcn_fdot2_f32_bf16(__builtin_bit_cast(bf16x2, a), __builtin_bit_cast(bf16x2, b), c, false);
}
DI f32x2 swp16(float a, float b) {
  auto r = __builtin_amdgcn_permlane16_swap(__float_as_uint(a), __float_as_uint(b), false, false);
  f32x2 v = {__uint_as_float(r[0]), __uint_as_float(r[1])}; return v;
}
DI f32x2 swp32(float a, float b) {
  auto r = __builtin_amdgcn_permlane32_swap(__float_as_uint(a), __float_as_uint(b), false, false);
  f32x2 v = {__uint_as_float(r[0]), __uint_as_float(r[1])}; return v;
}
DI float sum2rows(float a, float b) { f32x2 p = swp16(a, b); float s = p[0] + p[1]; f32x2 q = swp32(s, s); return q[0] + q[1]; }
template <int CTRL> DI float dppf(float v) { return __int_as_float(__builtin_amdgcn_update_dpp(0, __float_as_int(v), CTRL, 0xf, 0xf, true)); }
DI u32x4 swap_pair(unsigned a0, unsigned a1, unsigned b0, unsigned b1) {
  auto r0 = __builtin_amdgcn_permlane16_swap(a0, b0, false, false);
  auto r1 = __builtin_amdgcn_permlane16_swap(a1, b1, false, false);
  u32x4 v = {r0[0], r1[0], r0[1], r1[1]};
  return v;
}
DI void publish(char* ws, unsigned off, u64 tag, int tid) {
  asm volatile("s_waitcnt vmcnt(0)" ::: "memory");
  bar();
  if (tid == 0) __hip_atomic_store((GLB u64*)(ws + off), tag, __ATOMIC_RELAXED, __HIP_MEMORY_SCOPE_AGENT);
}
DI void poll(char* ws, unsigned off, int n, u64 tag, int lane) {
  GLB u64* p = (GLB u64*)(ws + off) + (lane < n ? lane : 0);
#pragma unroll 1
  for (int spin = 0; spin < (1 << 20); ++spin) {
    u64 v = __hip_atomic_load(p, __ATOMIC_RELAXED, __HIP_MEMORY_SCOPE_AGENT);
    if (__all(v == tag)) break;
    __builtin_amdgcn_s_sleep(1);
  }
  asm volatile("" ::: "memory");
}

DI float fma1(float a, float b, float c) { float d = __builtin_fmaf(a, b, c); asm("" : "+v"(d)); return d; }
DI float mul1(float a, float b) { float d = a * b; asm("" : "+v"(d)); return d; }
#define MF "v_mfma_f32_16x16x32_bf16 "
template <int RD> DI void cstep(f32x4& a, f32x4& b, bf16x8& h, bf16x8 wa, bf16x8 wb, unsigned ad, int off, int acc) {
  if (RD) {
    if (acc) asm volatile(MF "%0, %3, %2, %0\n\t" MF "%1, %4, %2, %1\n\tds_read_b128 %2, %5 offset:%6" : "+v"(a), "+v"(b), "+v"(h) : "v"(wa), "v"(wb), "v"(ad), "n"(off) : "memory");
    else asm volatile(MF "%0, %3, %2, 0\n\t" MF "%1, %4, %2, 0\n\tds_read_b128 %2, %5 offset:%6" : "=&v"(a), "=&v"(b), "+v"(h) : "v"(wa), "v"(wb), "v"(ad), "n"(off) : "memory");
  } else {
    if (acc) asm volatile(MF "%0, %3, %2, %0\n\t" MF "%1, %4, %2, %1" : "+v"(a), "+v"(b) : "v"(h), "v"(wa), "v"(wb));
    else asm volatile(MF "%0, %3, %2, 0\n\t" MF "%1, %4, %2, 0" : "=&v"(a), "=&v"(b) : "v"(h), "v"(wa), "v"(wb));
  }
}
DI void mstep(f32x4 (&c)[4], const bf16x8 (&wt)[4], u32x4 a, int acc) {
  if (acc) asm volatile(MF "%0, %4, %8, %0\n\t" MF "%1, %5, %8, %1\n\t" MF "%2, %6, %8, %2\n\t" MF "%3, %7, %8, %3"
                        : "+v"(c[0]), "+v"(c[1]), "+v"(c[2]), "+v"(c[3]) : "v"(wt[0]), "v"(wt[1]), "v"(wt[2]), "v"(wt[3]), "v"(a));
  else asm volatile(MF "%0, %4, %8, 0\n\t" MF "%1, %5, %8, 0\n\t" MF "%2, %6, %8, 0\n\t" MF "%3, %7, %8, 0"
                    : "=&v"(c[0]), "=&v"(c[1]), "=&v"(c[2]), "=&v"(c[3]) : "v"(wt[0]), "v"(wt[1]), "v"(wt[2]), "v"(wt[3]), "v"(a));
}
constexpr int tl(int p) { return (p & 1) * 4 + (p >> 1); }
constexpr unsigned ring(int t) { return t % 3 == 0 ? 65536u : t % 3 == 1 ? 98304u : 0u; }
constexpr unsigned warea(int j) { return j == 0 || j == 5 ? 32768u : j == 4 ? 131072u : j == 6 ? 0u : ring(7 + j); }

__global__ __launch_bounds__(512, 2) void fused_block(const u16* __restrict__ x, const u16* __restrict__ wqkv,
                                                      const u16* __restrict__ wo, const u16* __restrict__ wg,
                                                      const u16* __restrict__ wu, const u16* __restrict__ wd,
                                                      u16* __restrict__ out, char* __restrict__ ws) {
  const int tid = threadIdx.x, lane = tid & 63;
  const unsigned w = rfl(tid >> 6);
  const unsigned l15 = lane & 15, g = lane >> 4, q = (lane >> 2) & 3, p4 = lane & 3;
  const unsigned b = blockIdx.x, xcd = b & 7, bi = b >> 3;
  const rsrc_t rWS = mk(ws, WS_END);
  TS(0);
  const unsigned rt = xcd * 4 + (bi & 3), cg = bi >> 2;
  const unsigned panel = (xcd >> 1) * 2 + (bi & 1), fg = (xcd & 1) * 14 + (bi >> 1);
  const bool p3on = bi < 28;
  const unsigned prow = lane >> 3, pp = lane & 7;
  const unsigned fN = ((prow >> 1) & 1) | ((w & 1) << 1);
  const unsigned swzN = (((pp >> 1) ^ fN) << 5) | ((pp & 1) << 4);
  const unsigned frN = ((q >> 1) & 1) | ((g & 1) << 1);
  const unsigned trowN = (8 * g + q) * 128 + p4 * 8;
  const u32x4 ones = {0x3f803f80u, 0x3f803f80u, 0x3f803f80u, 0x3f803f80u};
  const rsrc_t rX = mk(x, S_ * D_ * 2);
  const unsigned vW3 = (8 * w + prow) * (F_ * 2) + (p3on ? fg : 0) * 128 + swzN;

  u64 tag, epoch;
  unsigned f1;
  {
    const unsigned rg = (xcd >> 1) * 8 + (bi & 7), hg = (xcd & 1) * 4 + (bi >> 3);
    const unsigned rot = ((bi & 7) + 2 * (bi >> 3)) & 7;
    const rsrc_t rW = mk(wqkv, D_ * 1536 * 2), rO = mk(wo, D_ * D_ * 2), rU = mk(wu, D_ * F_ * 2);
    u32x4 xrow;
    ld16(xrow, lane * 16, rX, (rg * 64 + hg * 8 + w) * 1024);
    const int r0 = 8 * w + prow;
    const unsigned vX0 = max((int)rg * 64 - 16 + r0, 0) * 1024 + ((pp ^ ((r0 >> 1) & 7)) << 4);
    const unsigned vX1 = (rg * 64 + 48 + 2 * w + (prow & 1)) * 1024 + ((pp ^ w) << 4);
    const unsigned vW = (8 * w + prow) * 3072 + hg * 128 + swzN;
    const unsigned vO = (8 * w + prow) * 1024 + cg * 128 + swzN;
    const u64 mX = w == 0 ? ~0xffull : ~0ull;
    auto piece = [&](int G, int i) {
      if (G < 8) {
        const unsigned st = (G + rot) & 7, sb = (G & 3) * SLOT;
        if (i == 0) dma_m(sb + w * 1024, vX0, rX, st * 128, mX);
        else if (i == 1) dma_m(sb + 8192 + w * 256, vX1, rX, st * 128, 0xffffull);
        else dma(sb + 10240 + (i - 2) * 8192 + w * 1024, vW, rW, st * 64 * 3072 + (i - 2) * 1024);
      } else if (i == 4) {
      } else if (G < 10) {
        dma((G - 8) * 32768 + (w + 8 * i) * 1024, vO, rO, (256 * (G - 8) + 64 * i) * 1024);
      } else {
        dma(L_UP + (w + 8 * i + 32 * (G - 10)) * 1024, vW3, rU, (64 * i + 256 * (G - 10)) * (F_ * 2));
      }
    };
#pragma unroll
    for (int i = 0; i < 5; ++i) piece(0, i);
    asm volatile("s_barrier" ::: "memory");
#pragma unroll
    for (int i = 0; i < 5; ++i) piece(1, i);
    TS(20);
    if (tid < 32) *(LDSP u32x4*)(smem + (tid >> 3) * SLOT + (tid & 7) * 16) = ones;
    epoch = *(const CST u64*)(ws + WS_EPOCH);
    tag = epoch * 0x9E3779B97F4A7C15ull + 0x632BE59BD9B4E019ull;
    const unsigned tag32 = (unsigned)(tag >> 32);
    {
      vm_tie<10>(xrow);
      TS(21);
      float s1 = 0.f, s2 = 0.f;
#pragma unroll
      for (int j = 0; j < 4; ++j) { s1 = dot2(xrow[j], 0x3f803f80u, s1); s2 = dot2(xrow[j], xrow[j], s2); }
      float s = sum2rows(s1, s2);
      s += dppf<0x128>(s); s += dppf<0x124>(s); s += dppf<0x122>(s); s += dppf<0x121>(s);
      const float m = __int_as_float(__builtin_amdgcn_readlane(__float_as_int(s), 0)) * (1.f / 512.f);
      const float e2 = __int_as_float(__builtin_amdgcn_readlane(__float_as_int(s), 16)) * (1.f / 512.f);
      const float rs = __builtin_amdgcn_rsqf(fmaxf(e2 - m * m, 0.f) + 1e-5f);
      const u32x4 gr = {__float_as_uint(m), tag32, __float_as_uint(rs), tag32};
      st16<16>(gr, rWS, lane == 0 ? 0u : 0x7ff00000u, WS_S1 + (rg * 64 + hg * 8 + w) * 16);
    }
    const unsigned hw = w & 3, half = w >> 2;
    const unsigned xa = (l15 * 128 + ((g ^ (l15 >> 1)) << 4) + half * 4096) ^ (half * 64);
    const unsigned wa = (10240 + trowN + ((hw ^ frN) << 5)) + half * 4096;
    const unsigned xb = xa ^ 64, wb = wa + 4096 - half * 8192;
    const unsigned par = l15 & 1;
    f32x4 mc, mp, mp1;
#pragma unroll
    for (int r = 0; r < 4; ++r) {
      const bool pm = (r & 1) == par, cur = 4 * g + r <= l15;
      mc[r] = (pm && cur) ? 0.f : -4e9f;
      mp[r] = (pm && !cur) ? 0.f : -4e9f;
      mp1[r] = (rg == 0 && half == 0) ? -4e9f : mp[r];
    }
    const unsigned se0 = par ? 0x01000c0cu : 0x0c0c0100u, se1 = par ? 0x03020c0cu : 0x0c0c0302u;
    const bool c0 = (4 * g + par) <= l15, c1 = (4 * g + 2 + par) <= l15;
    u32x4 sel = {c0 ? 0x0c0c0c0cu : se0, c1 ? 0x0c0c0c0cu : se1, c0 ? se0 : 0x0c0c0c0cu, c1 ? se1 : 0x0c0c0c0cu};
    unsigned vS = WS_S1 + max((int)(rg * 64 - 16 + half * 32 + (g < 3 ? g : 0) * 16 + l15), 0) * 16;
    tie(mc); tie(mp); tie(mp1); tie(sel); tie(vS);

    f32x4 aQ[2], aK[3], aV[3], aC = {0, 0, 0, 0};
#pragma unroll
    for (int i = 0; i < 3; ++i) { aK[i] = {0, 0, 0, 0}; aV[i] = {0, 0, 0, 0}; }
    aQ[0] = {0, 0, 0, 0}; aQ[1] = {0, 0, 0, 0};
    bf16x8 xf[2][2][3];
    s16x4 wl[2][2][3], wh[2][2][3];
    auto rd = [&](int s, int r) {
      const int kk = r / 9, i = r % 9;
      const unsigned sb = (s & 3) * SLOT;
      if (i < 3) xf[s & 1][kk][i] = lds_b128(sb + (kk ? xb : xa) + i * 2048);
      else {
        const int m = ((i - 3) / 2 + 1) % 3;
        const unsigned a = sb + (kk ? wb : wa) + m * 8192;
        if ((i - 3) & 1) wh[s & 1][kk][m] = lds_tr(a + 512); else wl[s & 1][kk][m] = lds_tr(a);
      }
    };
    auto mm = [&](int s, int n) {
      const int c = s & 1, kk = n > 8, i = kk ? n - 9 : n;
      const bf16x8 wq = cat(wl[c][kk][0], wh[c][kk][0]), wk = cat(wl[c][kk][1], wh[c][kk][1]), wv = cat(wl[c][kk][2], wh[c][kk][2]);
      SB();
      if (i < 3) aK[i] = mfma32(wk, xf[c][kk][i], aK[i]);
      else if (i < 6) aV[i - 3] = mfma32(xf[c][kk][i - 3], wv, aV[i - 3]);
      else if (i < 8) aQ[i - 6] = mfma32(wq, xf[c][kk][i - 5], aQ[i - 6]);
      else aC = mfma32(wq, asbf(ones), aC);
      SB();
    };
    TS(22);
    wait_vm<6>();
    TS(23);
    bar();
    TS(1);
#pragma unroll
    for (int r = 0; r < 18; ++r) rd(0, r);
    SB();
#pragma unroll
    for (int i = 0; i < 5; ++i) piece(2, i);
    u32x4 sl;
    SB();
    auto part = [&](int s, int n0, int n1) __attribute__((always_inline)) {
#pragma unroll
      for (int n = n0; n < n1; ++n) {
        mm(s, n);
        const int slot = n % 3 == 2 ? n / 3 + (n > 13) : n == 13 ? 4 : n == 16 ? 6 : -1;
        const int first = s == 0 ? 15 : s == 1 ? 22 : s == 2 ? 29 : 5 * s + 20, cnt = s < 2 ? 7 : s == 2 ? 6 : 5;
        if (slot >= 0 && slot < cnt) piece((first + slot) / 5, (first + slot) % 5);
        if (n % 3 != 2 && n < 13 && s < 7) { rd(s + 1, 2 * (n - n / 3)); rd(s + 1, 2 * (n - n / 3) + 1); }
      }
    };
#pragma unroll
    for (int s = 0; s < 6; ++s) {
      if (s == 0) wait_vm<6>(); else if (s == 1) wait_vm<7>(); else if (s == 2) wait_vm<9>(); else wait_vm<10>();
      bar();
      if (s == 4) { TS(2); ld16c<0>(sl, vS, rWS, 0); }
      part(s, 0, 17);
    }
    vm_tie<8>(sl);
    bar();
    part(6, 0, 14);
#pragma unroll 1
    for (int spin = 0; spin < (1 << 20); ++spin) {
      if (__all(sl[1] == tag32 && sl[3] == tag32)) break;
      __builtin_amdgcn_s_sleep(1);
      ld16c<0>(sl, vS, rWS, 0);
      vm_tie<0>(sl);
    }
    *(LDSP unsigned*)(smem + L_FS + w * 512 + lane * 4) = sl[0];
    *(LDSP unsigned*)(smem + L_FS + w * 512 + 256 + lane * 4) = sl[2];
    part(6, 14, 17);
    bar();
    part(7, 0, 17);
    TS(3);
    *(LDSP f32x4*)(smem + L_CX + w * 2304 + lane * 16) = aC;
    *(LDSP f32x4*)(smem + L_CX + w * 2304 + 1024 + lane * 16) = aK[0];
    *(LDSP float*)(smem + L_CX + w * 2304 + 2048 + lane * 4) = aV[0][0];
    float sm[3], sr[3];
    f32x4 vm[3], vr[3];
#pragma unroll
    for (int t = 0; t < 3; ++t) {
      sm[t] = *(LDSP float*)(smem + L_FS + w * 512 + t * 64 + l15 * 4);
      sr[t] = *(LDSP float*)(smem + L_FS + w * 512 + 256 + t * 64 + l15 * 4);
      vm[t] = *(LDSP f32x4*)(smem + L_FS + w * 512 + t * 64 + g * 16);
      vr[t] = *(LDSP f32x4*)(smem + L_FS + w * 512 + 256 + t * 64 + g * 16);
    }
    wait_vm<8>();
    bar();
    const f32x4 csq = aC + *(LDSP f32x4*)(smem + L_CX + (w ^ 4) * 2304 + lane * 16);
    const f32x4 csk = *(LDSP f32x4*)(smem + L_CX + hw * 2304 + 1024 + g * 256);
    const float csv = *(LDSP float*)(smem + L_CX + hw * 2304 + 2048 + l15 * 4);
    u32x2 qb[2], kb[3], vb[3];
#pragma unroll
    for (int t = 0; t < 3; ++t) {
      const f32x4 kv = (aK[t] - sm[t] * csk) * sr[t];
      kb[t] = {pk(kv[0], kv[1]), pk(kv[2], kv[3])};
      if (t > 0) { const f32x4 qv = (aQ[t - 1] - sm[t] * csq) * sr[t]; qb[t - 1] = {pk(qv[0], qv[1]), pk(qv[2], qv[3])}; }
      const f32x4 vv = (aV[t] - vm[t] * csv) * vr[t];
      vb[t] = {pk(vv[0], vv[1]), pk(vv[2], vv[3])};
    }
    float l0[2], l1[2];
#pragma unroll
    for (int t = 0; t < 2; ++t) {
      const f32x4 sc = mfma16(kb[t + 1], qb[t], mc), sp = mfma16(kb[t], qb[t], t ? mp : mp1);
      l0[t] = fmaxf(fmaxf(fmaxf(sc[0], sc[1]), sp[0]), sp[1]);
      l1[t] = fmaxf(fmaxf(fmaxf(sc[2], sc[3]), sp[2]), sp[3]);
    }
    f32x2 mx;
    {
      const f32x2 a = swp16(fmaxf(l0[0], l1[0]), fmaxf(l0[1], l1[1]));
      const float m1 = fmaxf(a[0], a[1]);
      const f32x2 c = swp32(m1, m1);
      const float m2 = fmaxf(c[0], c[1]) * (-0.25f * 1.44269504f);
      mx = swp16(m2, m2);
    }
    unsigned ob[2][2];
#pragma unroll
    for (int t = 0; t < 2; ++t) {
      const float e0 = __builtin_amdgcn_exp2f(fmaf(l0[t], 0.25f * 1.44269504f, mx[t])), e1 = __builtin_amdgcn_exp2f(fmaf(l1[t], 0.25f * 1.44269504f, mx[t]));
      const unsigned e = pk(e0, e1);
      u32x4 pf;
#pragma unroll
      for (int j = 0; j < 4; ++j) pf[j] = __builtin_amdgcn_perm(0u, e, sel[j]);
      const u32x4 vf = {vb[t][0], vb[t][1], vb[t + 1][0], vb[t + 1][1]};
      const f32x4 z = {0, 0, 0, 0};
      const f32x4 den = mfma32(asbf(ones), asbf(pf), z);
      const f32x4 o = mfma32(asbf(vf), asbf(pf), z) * __builtin_amdgcn_rcpf(den[0]);
      ob[t][0] = pk(o[0], o[1]); ob[t][1] = pk(o[2], o[3]);
    }
    {
      const u32x4 ov = swap_pair(ob[0][0], ob[0][1], ob[1][0], ob[1][1]);
      const unsigned head = hg * 4 + hw;
      const unsigned off = (g & 1) * 16384 + ((g >> 1) * 16 + l15) * 16;
      st16<16>(ov, rWS, off, WS_O + ((rg * 4 + half * 2) * 16 + (head >> 1)) * 1024 + (head & 1) * 512);
    }
    TS(4);
    f1 = WS_F1 + (rg * 16 + hg) * 8;
  }

  const unsigned c4 = w & 3, hh = w >> 2;
  bf16x8 fgt[16], fup[16];
  {
    const unsigned gp = ((g & 1) << 1) | (g >> 1);
    const unsigned m2 = (w >> 2) * 2 + (w & 1), kh = (w >> 1) & 1;
    u32x4 xr;
    ld16(xr, (m2 * 16 + l15) * 1024 + gp * 16, rX, rt * 65536 + cg * 128 + kh * 64);
    bf16x8 wf[8][4];
#pragma unroll
    for (int ks = 0; ks < 8; ++ks)
#pragma unroll
      for (int c = 0; c < 4; ++c) {
        const unsigned a = kh * 32768 + trowN + ((c ^ (2 * kh) ^ frN) << 5) + ks * 4096;
        wf[ks][c] = lds_tr2(a, a + 512);
      }
    SB();
    vm_tie<0>(xr);
    publish(ws, f1, tag, tid);
    TS(5);
    {
      const rsrc_t rG = mk(wg, D_ * F_ * 2);
#pragma unroll
      for (int i = 0; i < 8; ++i) dma(L_GATE + (w + 8 * i) * 1024, vW3, rG, 64 * i * (F_ * 2));
    }
    TS(6);
    poll(ws, WS_F1 + (rt * 16 + kh * 4) * 8, 4, tag, lane);
    TS(7);
    u32x4 of[8];
    const unsigned ob = WS_O + ((rt * 4 + m2) * 16 + kh * 8) * 1024;
    ld16c<0>(of[0], lane * 16, rWS, ob); ld16c<1024>(of[1], lane * 16, rWS, ob); ld16c<2048>(of[2], lane * 16, rWS, ob); ld16c<3072>(of[3], lane * 16, rWS, ob);
    ld16c<0>(of[4], lane * 16, rWS, ob + 4096); ld16c<1024>(of[5], lane * 16, rWS, ob + 4096); ld16c<2048>(of[6], lane * 16, rWS, ob + 4096); ld16c<3072>(of[7], lane * 16, rWS, ob + 4096);
    f32x4 acc[4];
#pragma unroll
    for (int c = 0; c < 4; ++c) acc[c] = {0, 0, 0, 0};
    auto step = [&](int ks) {
#pragma unroll
      for (int c = 3; c >= 0; --c) acc[c] = mfma32(wf[ks][c], asbf(of[ks]), acc[c]);
      SB();
    };
    vm_tie<7>(of[0]); step(0); vm_tie<6>(of[1]); step(1); vm_tie<5>(of[2]); step(2); vm_tie<4>(of[3]); step(3);
    vm_tie<3>(of[4]); step(4); vm_tie<2>(of[5]); step(5); vm_tie<1>(of[6]); step(6); vm_tie<0>(of[7]); step(7);
    TS(8);
    *(LDSP f32x4*)(smem + L_RED + w * 2048 + lane * 16) = acc[2];
    *(LDSP f32x4*)(smem + L_RED + w * 2048 + 1024 + lane * 16) = acc[3];
    bar();
    const f32x4 k0 = acc[0] + *(LDSP f32x4*)(smem + L_RED + (w ^ 2) * 2048 + lane * 16);
    const f32x4 k1 = acc[1] + *(LDSP f32x4*)(smem + L_RED + (w ^ 2) * 2048 + 1024 + lane * 16);
    const u32x4 av = swap_pair(pk(k0[0], k0[1]), pk(k0[2], k0[3]), pk(k1[0], k1[1]), pk(k1[2], k1[3]));
    u32x4 hreg;
#pragma unroll
    for (int j = 0; j < 4; ++j) hreg[j] = pk(blo(av[j]) + blo(xr[j]), bhi(av[j]) + bhi(xr[j]));
    st16<16>(hreg, rWS, (gp * 16 + l15) * 16, WS_H + ((rt * 4 + m2) * 16 + cg * 2 + kh) * 1024);
    float s1 = 0.f, s2 = 0.f;
#pragma unroll
    for (int j = 0; j < 4; ++j) { s1 = dot2(hreg[j], 0x3f803f80u, s1); s2 = dot2(hreg[j], hreg[j], s2); }
    const float ss = sum2rows(s1, s2);
    __builtin_amdgcn_raw_buffer_store_b32(__float_as_uint(ss), rWS, g < 2 ? l15 * 8 + g * 4 : 0x7ff00000u, WS_S2 + ((rt * 4 + m2) * 16 + cg * 2 + kh) * 128, 16);
    SB();
    const unsigned a = trowN + ((c4 ^ frN) << 5);
#pragma unroll
    for (int ks = 0; ks < 16; ++ks) fup[ks] = lds_tr2(L_UP + a + ks * 4096, L_UP + a + ks * 4096 + 512);
    SB();
    publish(ws, WS_F2 + (rt * 16 + cg) * 8, tag, tid);
    TS(9);
#pragma unroll
    for (int ks = 0; ks < 16; ++ks) fgt[ks] = lds_tr2(L_GATE + a + ks * 4096, L_GATE + a + ks * 4096 + 512);
    SB();
  }

  const rsrc_t rD = mk(wd, D_ * F_ * 2);
  const unsigned vD0 = prow * 1024 + cg * 128 + ((((pp >> 1) ^ ((prow >> 1) & 1)) << 5) | ((pp & 1) << 4)), vD1 = vD0 ^ 64;
  const unsigned lw = hh * 16384 + c4 * 1024;
  auto dma_wd = [&](int j, int i) { dma(warea(j) + i * 4096 + lw, (i & 1) ? vD1 : vD0, rD, w * (7 * 32 * 1024) + (j * 32 + i * 8) * 1024); };
  bf16x8 wdf[7][4];
  unsigned tw[4];
  auto mktw = [&](unsigned base) {
    unsigned ln = lane; tie(ln);
    const unsigned t = base + lw + (ln >> 4) * 4096 + ((ln >> 2) & 3) * 128 + (ln & 3) * 8 + ((((ln >> 3) & 1) | ((ln >> 3) & 2)) << 5);
#pragma unroll
    for (int c = 0; c < 4; ++c) { tw[c] = t ^ (c << 5); tie(tw[c]); }
  };
  auto rdw = [&](int j, int c) { const unsigned a = tw[c] + (warea(j) & 65535); wdf[j][c] = lds_tr2(a, a + 512); };
  const unsigned sF = WS_F3 + ((rt >> 1) * 128 + w * 14) * 8;
  u32x2 fl;
  auto ldf = [&]() { unsigned ln = lane; tie(ln); ld8c<0>(fl, min(ln, 13u) * 8, rWS, sF); };
  if (p3on) {
    TS(10);
    poll(ws, WS_F2 + ((panel * 4 + 2 * hh + ((lane >> 3) & 1)) * 16) * 8, 8, tag, lane & 7);
    TS(11);
    const unsigned hb = WS_H + (panel * 16 + hh * 8) * 16384 + c4 * 1024;
    auto dma_t = [&](int T, int i) {
      if (T < 8) dmac(ring(T) + i * 4096 + lw, lane * 16, rWS, hb + tl(T) * 16384 + i * 4096);
      else dma_wd(T - 7, i);
    };
    u32x2 sp[2][4];
#pragma unroll
    for (int i = 0; i < 4; ++i) dma_t(0, i);
#pragma unroll
    for (int t = 0; t < 2; ++t) {
      const unsigned so = WS_S2 + (panel * 16 + 2 * w + t) * 2048, vs = g * 512 + l15 * 8;
      ld8c<0>(sp[t][0], vs, rWS, so); ld8c<128>(sp[t][1], vs, rWS, so); ld8c<256>(sp[t][2], vs, rWS, so); ld8c<384>(sp[t][3], vs, rWS, so);
    }
    f32x4 csg = {0, 0, 0, 0}, csu = {0, 0, 0, 0};
#pragma unroll
    for (int ks = 0; ks < 16; ++ks) {
      SB(); csu = mfma32(fup[ks], asbf(ones), csu); csg = mfma32(fgt[ks], asbf(ones), csg); SB();
      if ((ks & 3) == 0) dma_t(1, ks >> 2);
    }
    asm volatile("s_waitcnt vmcnt(4)" : "+v"(sp[0][0]), "+v"(sp[0][1]), "+v"(sp[0][2]), "+v"(sp[0][3]), "+v"(sp[1][0]), "+v"(sp[1][1]), "+v"(sp[1][2]), "+v"(sp[1][3]) :: "memory");
    bar();
    TS(12);
    unsigned ha = hh * 16384 + lane * 16, ha1 = ha + 65536;
    tie(ha); tie(ha1);
    bf16x8 hf[16];
    auto rdh = [&](int T, int f) { hf[f] = lds_b128((ring(T) ? ha1 : ha) + (ring(T) & 65535) + f * 1024); };
#pragma unroll
    for (int f = 0; f < 16; ++f) rdh(0, f);
    SB();
#pragma unroll
    for (int t = 0; t < 2; ++t) {
      dma_t(2, 2 * t); dma_t(2, 2 * t + 1);
      const f32x2 s = (__builtin_bit_cast(f32x2, sp[t][0]) + __builtin_bit_cast(f32x2, sp[t][1])) + (__builtin_bit_cast(f32x2, sp[t][2]) + __builtin_bit_cast(f32x2, sp[t][3]));
      const float r = sum2rows(s[0], s[1]) * (1.f / 512.f);
      const f32x2 me = swp16(r, r);
      const float var = fmaxf(me[1] - me[0] * me[0], 0.f) + 1e-5f;
      const f32x4 st = {-me[0], __builtin_amdgcn_rsqf(var) * -1.44269504f, var, 0.f};
      *(LDSP f32x4*)(smem + L_ST + (2 * w + t) * 256 + l15 * 16) = st;
    }
    wait_vm<4>();
    bar();
    TS(20 + 40);
    if (hh) {
#pragma unroll
      for (int i = 0; i < 3; ++i) {
        dma_t(3, i);
        dmac(ring(3) + i * 4096 + c4 * 1024, lane * 16, rWS, hb - 8 * 16384 + tl(3) * 16384 + i * 4096);
      }
      bar();
    }
    f32x4 ag, au, st;
    float gg[4], uu[4], ee[4];
    unsigned av[2][2];
    unsigned sa = L_ST + hh * 2048 + l15 * 16;
    unsigned ao = (g & 1) * (4 * 56 * 1024) + ((g >> 1) * 16 + l15) * 16;
    tie(sa); tie(ao);
    const unsigned ab = WS_A + ((panel * 16 + hh * 8) * 56 + fg * 2 + (c4 >> 1)) * 1024 + (c4 & 1) * 512;
    auto epi = [&](int p, int k) {
      const int e = p & 1;
      if (k < 4) {
        gg[k] = fma1(st[0], csg[k], ag[k]); const float a = mul1(gg[k], st[1]); uu[k] = fma1(st[0], csu[k], au[k]);
        ee[k] = __builtin_amdgcn_exp2f(a);
      } else if (k < 8) {
        const float d = fma1(ee[k - 4], st[2], st[2]);
        gg[k - 4] = mul1(gg[k - 4], uu[k - 4]);
        ee[k - 4] = __builtin_amdgcn_rcpf(d);
      } else if (k == 8) {
        av[e][0] = min(pk(mul1(gg[0], ee[0]), mul1(gg[1], ee[1])), ~1u); av[e][1] = min(pk(mul1(gg[2], ee[2]), mul1(gg[3], ee[3])), ~1u);
      } else if (e) {
        st16<16>(swap_pair(av[0][0], av[0][1], av[1][0], av[1][1]), rWS, ao, ab + (p >> 1) * (56 * 1024));
      } else if (p < 4) {
        u32x4 none = {~0u, ~0u, ~0u, ~0u};
        tie(none);
        st16<16>(none, rWS, ao, ab + (2 + (p >> 1)) * (56 * 1024));
      }
      SB();
    };
    st = *(LDSP f32x4*)(smem + sa);
    SB();
#pragma unroll
    for (int p = 0; p < 8; ++p) {
      if (p == 7) mktw(0);
      PRIO(1);
#pragma unroll
      for (int ks = 0; ks < 16; ++ks) {
        const unsigned hn = ring(p + 1) ? ha1 : ha;
        if (ks < 12 && p < 7) cstep<1>(ag, au, hf[ks], fgt[ks], fup[ks], hn, (ring(p + 1) & 65535) + ks * 1024, ks);
        else cstep<0>(ag, au, hf[ks], fgt[ks], fup[ks], hn, 0, ks);
        SB();
        if (ks < 8 && p == 7) rdw(ks >> 2, ks & 3);
        SB();
      }
      asm volatile("s_nop 4" : "+v"(ag), "+v"(au));
      if (p < 7) PRIO(0);
      if (p == 5) wait_vm<10>(); else if (p >= 2 && p < 7) wait_vm<11>();
      TS3(24 + 4 * p);
      if (p < 7 || !hh) bar();
      TS3(25 + 4 * p);
      if (p < 7) {
#pragma unroll
        for (int f = 12; f < 16; ++f) rdh(p + 1, f);
        SB();
        dma_t(p + 3, 3); epi(p, 0); epi(p, 1);
        dma_t(p + 4, 0); epi(p, 2); epi(p, 3);
        dma_t(p + 4, 1); epi(p, 4); epi(p, 5);
        dma_t(p + 4, 2); epi(p, 6); epi(p, 7);
        st = *(LDSP f32x4*)(smem + sa + tl(p + 1) * 256);
        SB();
        dma_wd(p & 4, p & 3);
        epi(p, 8); epi(p, 9);
        if (p == 6) ldf();
        if (p == 0) wait_vm<6>(); else if (p == 2 || p == 3) wait_vm<14>(); else if (p == 4) wait_vm<13>(); else if (p == 5) wait_vm<11>(); else if (p == 6) wait_vm<12>();
        if (p == 5) {
          unsigned ln = lane; tie(ln);
          const u32x2 tag2 = {(unsigned)tag, (unsigned)(tag >> 32)};
          __builtin_amdgcn_raw_buffer_store_b64(tag2, rWS, ln ? 0x7ff00000u : 0u, WS_F3 + ((panel * 2 + hh) * 128 + fg * 4 + c4) * 8, 16);
          SB();
        }
        TS3(26 + 4 * p);
        bar();
        TS3(27 + 4 * p);
      }
    }
    TS(13);
#pragma unroll
    for (int k = 0; k < 10; ++k) epi(7, k);
    PRIO(0);
    wait_lds();
    TS(14);
  } else {
    bar();
#pragma unroll
    for (int n = 0; n < 20; ++n) if (n != 15 && n != 19) dma_wd(n >> 2, n & 3);
    if (b == 255 && w == 0) {
#pragma unroll 1
      for (int j = 0; j < 4; ++j) {
        GLB u64* p = (GLB u64*)(ws + WS_F1) + (j * 8 + (lane >> 3)) * 16 + (lane & 7);
#pragma unroll 1
        for (int spin = 0; spin < (1 << 20); ++spin) {
          u64 v = __hip_atomic_load(p, __ATOMIC_RELAXED, __HIP_MEMORY_SCOPE_AGENT);
          if (__all(v == tag)) break;
          __builtin_amdgcn_s_sleep(1);
        }
      }
      if (lane == 0) __hip_atomic_store((GLB u64*)(ws + WS_EPOCH), epoch + 1, __ATOMIC_RELAXED, __HIP_MEMORY_SCOPE_AGENT);
    }
    wait_vm<0>();
    mktw(0);
#pragma unroll
    for (int n = 0; n < 8; ++n) rdw(n >> 2, n & 3);
    wait_lds();
#pragma unroll 1
    for (int spin = 0; spin < (1 << 18); ++spin) {
      ldf();
      vm_tie<0>(fl);
      if (__all(fl[0] == (unsigned)tag && fl[1] == (unsigned)(tag >> 32))) break;
      __builtin_amdgcn_s_sleep(10);
    }
  }

  {
    const unsigned ab = WS_A + (rt * 4 * 56 + w * 7) * 1024;
    u32x4 af[3][7];
    auto lda = [&](int t, int j) {
      const unsigned so = ab + t * (56 * 1024) + (j >> 2) * 4096;
      u32x4& a = af[t % 3][j];
      if ((j & 3) == 0) ld16c<0>(a, lane * 16, rWS, so); else if ((j & 3) == 1) ld16c<1024>(a, lane * 16, rWS, so);
      else if ((j & 3) == 2) ld16c<2048>(a, lane * 16, rWS, so); else ld16c<3072>(a, lane * 16, rWS, so);
    };
    TS(15);
    dma_wd(3, 3); dma_wd(4, 3);
#pragma unroll
    for (int n = 0; n < 8; ++n) dma_wd(5 + (n >> 2), n & 3);
    const unsigned fc = lane >> 4, fq = (lane >> 2) & 3, fr = (lane >> 1) & 1, fp = lane & 1;
    unsigned hv[4];
#pragma unroll
    for (int t = 0; t < 4; ++t)
      ld4c(hv[t], (fc >> 1) * 1024 + ((fc & 1) * 2 + (fq >> 1)) * 256 + fr * 16 + (fq & 1) * 8 + fp * 4, rWS, WS_H + ((rt * 4 + t) * 16 + cg * 2) * 1024 + w * 32);
    vm_tie<15>(fl);
    if (__builtin_expect(!__all(fl[0] == (unsigned)tag && fl[1] == (unsigned)(tag >> 32)), 0)) {
#pragma unroll 1
      for (int spin = 0; spin < (1 << 20); ++spin) {
        __builtin_amdgcn_s_sleep(1);
        ldf();
        vm_tie<0>(fl);
        if (__all(fl[0] == (unsigned)tag && fl[1] == (unsigned)(tag >> 32))) break;
      }
    }
#pragma unroll
    for (int j = 0; j < 7; ++j) lda(0, j);
    mktw(65536);
    wait_vm<19>();
#pragma unroll
    for (int j = 0; j < 7; ++j) {
      if (j == 4) mktw(131072);
      if (j < 6) { rdw(2 + (j >> 1), (j & 1) * 2); rdw(2 + (j >> 1), (j & 1) * 2 + 1); }
      SB();
      lda(1, j);
    }
    mktw(0);
    asm volatile("s_waitcnt vmcnt(14)" : "+v"(hv[0]), "+v"(hv[1]), "+v"(hv[2]), "+v"(hv[3]) :: "memory");
#pragma unroll
    for (int n = 20; n < 28; ++n) rdw(n >> 2, n & 3);
    SB();
    TS(16);
    f32x4 acc[2][4];
    unsigned top;
    auto mm = [&](int t, int j) {
      const u32x4 a = af[t % 3][j];
      mstep(acc[t & 1], wdf[j], a, j);
      if (t >= 2) { top = max(max(j ? top : 0u, a[0]), a[1]); top = max(max(top, a[2]), a[3]); tie(top); }
      SB();
    };
    auto redo = [&](int t) {
      if (__builtin_expect(__any(top == ~0u), 0)) {
#pragma unroll 1
        for (int spin = 0; spin < (1 << 20); ++spin) {
          __builtin_amdgcn_s_sleep(2);
#pragma unroll
          for (int j = 0; j < 7; ++j) lda(t, j);
          auto& a = af[t % 3];
          asm volatile("s_waitcnt vmcnt(0)" : "+v"(a[0]), "+v"(a[1]), "+v"(a[2]), "+v"(a[3]), "+v"(a[4]), "+v"(a[5]), "+v"(a[6]) :: "memory");
#pragma unroll
          for (int j = 0; j < 7; ++j) mm(t, j);
          if (!__any(top == ~0u)) break;
        }
      }
    };
    const unsigned pw0 = lw + (g * 16 + (l15 ^ (2 * g))) * 16, pw1 = pw0 ^ 128;
    auto put = [&](int t) {
      auto& a = acc[t & 1];
      asm volatile("s_nop 7" : "+v"(a[0]), "+v"(a[1]), "+v"(a[2]), "+v"(a[3]));
#pragma unroll
      for (int c = 0; c < 4; ++c) *(LDSP f32x4*)(smem + warea(t + 2) + c * 4096 + ((c & 1) ? pw1 : pw0)) = acc[t & 1][c];
      SB();
    };
    const unsigned pr = fc * 4096 + (fq * 16 + ((2 * w + fr) ^ (2 * fq + 8 * (fc & 1)))) * 16 + fp * 8;
    const rsrc_t rOut = mk(out, S_ * D_ * 2);
    const unsigned vOut = fr * 1024 + fc * 32 + fq * 8 + fp * 4;
    auto fin = [&](int t) {
      f32x2 v[8];
#pragma unroll
      for (int s = 0; s < 8; ++s) v[s] = *(LDSP f32x2*)(smem + warea(t + 2) + pr + (s >> 2) * 16384 + (s & 3) * 1024);
      const f32x2 r = ((v[0] + v[1]) + (v[2] + v[3])) + ((v[4] + v[5]) + (v[6] + v[7]));
      const unsigned f = pk(r[0], r[1]);
      __builtin_amdgcn_raw_buffer_store_b32(pk(blo(f) + blo(hv[t]), bhi(f) + bhi(hv[t])), rOut, vOut, (rt * 64 + t * 16 + 2 * w) * 1024 + cg * 128, 0);
    };
#pragma unroll
    for (int j = 0; j < 7; ++j) { lda(2, j); vm_tie<14>(af[0][j]); mm(0, j); }
    TS(61);
#pragma unroll
    for (int j = 0; j < 7; ++j) { lda(3, j); vm_tie<14>(af[1][j]); mm(1, j); if (j == 0) put(0); }
    TS(62);
    vm_tie<13>(af[2][0]); mm(2, 0); put(1); vm_tie<12>(af[2][1]); mm(2, 1); vm_tie<11>(af[2][2]); mm(2, 2); vm_tie<10>(af[2][3]); mm(2, 3);
    vm_tie<9>(af[2][4]); mm(2, 4); vm_tie<8>(af[2][5]); mm(2, 5); vm_tie<7>(af[2][6]); mm(2, 6);
    redo(2);
    put(2);
    TS(63);
    bar();
    fin(0); fin(1); fin(2);
    SB();
    vm_tie<9>(af[0][0]); mm(3, 0); vm_tie<8>(af[0][1]); mm(3, 1); vm_tie<7>(af[0][2]); mm(3, 2); vm_tie<6>(af[0][3]); mm(3, 3);
    vm_tie<5>(af[0][4]); mm(3, 4); vm_tie<4>(af[0][5]); mm(3, 5); vm_tie<3>(af[0][6]); mm(3, 6);
    redo(3);
    TS(17);
    put(3);
    bar();
    fin(3);
    TS(18);
  }
}

extern "C" void kernel_launch(void* const* d_in, const int* in_sizes, int n_in, void* d_out, int out_size, void* d_ws,
                              size_t ws_size, hipStream_t stream) {
  (void)hipFuncSetAttribute((const void*)fused_block, hipFuncAttributeMaxDynamicSharedMemorySize, LDS_BYTES);
  fused_block<<<256, 512, LDS_BYTES, stream>>>((const u16*)d_in[0], (const u16*)d_in[1], (const u16*)d_in[2], (const u16*)d_in[4],
                                               (const u16*)d_in[5], (const u16*)d_in[6], (u16*)d_out, (char*)d_ws);
}
```

```cpp
#include <hip/hip_runtime.h>
#include <hip/hip_bf16.h>

typedef unsigned short u16;
typedef unsigned long long u64;
typedef __attribute__((ext_vector_type(8))) __bf16 bf16x8;
typedef __attribute__((ext_vector_type(2))) __bf16 bf16x2;
typedef __attribute__((ext_vector_type(4))) float f32x4;
typedef __attribute__((ext_vector_type(2))) float f32x2;
typedef __attribute__((ext_vector_type(16))) float f32x16;
typedef __attribute__((ext_vector_type(4))) short s16x4;
typedef __attribute__((ext_vector_type(8))) short s16x8;
typedef __attribute__((ext_vector_type(4))) unsigned u32x4;
typedef __attribute__((ext_vector_type(2))) unsigned u32x2;
#define LDSP __attribute__((address_space(3)))
#define GLB __attribute__((address_space(1)))
#define CST __attribute__((address_space(4)))
#define DI __device__ __forceinline__
#define SB() __builtin_amdgcn_sched_barrier(0)
typedef __amdgpu_buffer_rsrc_t rsrc_t;

constexpr int S_ = 2048, D_ = 512, F_ = 1792;
constexpr unsigned WS_EPOCH = 0;
constexpr unsigned WS_F1 = 4096;
constexpr unsigned WS_F2 = WS_F1 + 4096;
constexpr unsigned WS_S1 = 16384;
constexpr unsigned WS_F3 = WS_F2 + 4096;
constexpr unsigned WS_O = 131072;
constexpr unsigned WS_H = WS_O + 2097152;
constexpr unsigned WS_A = WS_H + 2097152;
constexpr unsigned WS_S2 = WS_A + 128 * 56 * 1024;
constexpr unsigned WS_END = WS_S2 + 128 * 2048;
constexpr unsigned SLOT = 34816;
constexpr unsigned L_SP = 4 * SLOT;
constexpr unsigned L_FS = L_SP;
constexpr unsigned L_CX = L_SP + 4096;
constexpr unsigned L_RED = L_SP + 4096;
constexpr unsigned L_ST = 32768;
constexpr unsigned LDS_BYTES = 163840;

extern __shared__ __attribute__((aligned(16))) char smem[];
#define TS(i)
#define TS3(i)
#define PRIO(x) do { SB(); __builtin_amdgcn_s_setprio(x); SB(); } while (0)

DI unsigned rfl(unsigned v) { return __builtin_amdgcn_readfirstlane(v); }
DI rsrc_t mk(const void* p, unsigned bytes) { return __builtin_amdgcn_make_buffer_rsrc((void*)p, (short)0, (int)bytes, 0x00020000); }
DI void dma(unsigned lds, unsigned voff, rsrc_t r, unsigned soff) {
  asm volatile("s_mov_b32 m0, %0\n\ts_nop 0\n\tbuffer_load_dwordx4 %1, %2, %3 offen lds"
               :: "s"(lds + (unsigned)(size_t)(LDSP char*)smem), "v"(voff), "s"(r), "s"(soff) : "memory");
}
DI void dma_m(unsigned lds, unsigned voff, rsrc_t r, unsigned soff, u64 mask) {
  asm volatile("s_mov_b32 m0, %0\n\ts_mov_b64 exec, %4\n\tbuffer_load_dwordx4 %1, %2, %3 offen lds\n\ts_mov_b64 exec, -1"
               :: "s"(lds + (unsigned)(size_t)(LDSP char*)smem), "v"(voff), "s"(r), "s"(soff), "s"(mask) : "memory");
}
DI void dmac(unsigned lds, unsigned voff, rsrc_t r, unsigned soff) {
  asm volatile("s_mov_b32 m0, %0\n\ts_nop 0\n\tbuffer_load_dwordx4 %1, %2, %3 offen sc1 lds"
               :: "s"(lds + (unsigned)(size_t)(LDSP char*)smem), "v"(voff), "s"(r), "s"(soff) : "memory");
}
template <int OFF> DI void ld16c(u32x4& d, unsigned voff, rsrc_t r, unsigned soff) {
  asm volatile("buffer_load_dwordx4 %0, %1, %2, %3 offen offset:%4 sc1" : "=v"(d) : "v"(voff), "s"(r), "s"(soff), "n"(OFF) : "memory");
}
template <int OFF> DI void ld8c(u32x2& d, unsigned voff, rsrc_t r, unsigned soff) {
  asm volatile("buffer_load_dwordx2 %0, %1, %2, %3 offen offset:%4 sc1" : "=v"(d) : "v"(voff), "s"(r), "s"(soff), "n"(OFF) : "memory");
}
DI void ld4c(unsigned& d, unsigned voff, rsrc_t r, unsigned soff) {
  asm volatile("buffer_load_dword %0, %1, %2, %3 offen sc1" : "=v"(d) : "v"(voff), "s"(r), "s"(soff) : "memory");
}
DI void ld16(u32x4& d, unsigned voff, rsrc_t r, unsigned soff) {
  asm volatile("buffer_load_dwordx4 %0, %1, %2, %3 offen" : "=v"(d) : "v"(voff), "s"(r), "s"(soff) : "memory");
}
DI void touch(unsigned voff, rsrc_t r) {
  asm volatile("buffer_load_dword v255, %0, %1, 0 offen" :: "v"(voff), "s"(r) : "memory", "v255");
}
template <int AUX> DI void st16(u32x4 v, rsrc_t r, unsigned voff, unsigned soff) {
  __builtin_amdgcn_raw_buffer_store_b128(v, r, voff, soff, AUX);
  asm volatile("s_nop 1" :: "v"(v) : "memory");
}
template <int N> DI void wait_vm() { asm volatile("s_waitcnt vmcnt(%0)" :: "n"(N) : "memory"); }
template <int N, class T> DI void vm_tie(T& a) { asm volatile("s_waitcnt vmcnt(%1)" : "+v"(a) : "n"(N) : "memory"); }
template <int N, class T> DI void vm_tie(T& a, T& b) { asm volatile("s_waitcnt vmcnt(%2)" : "+v"(a), "+v"(b) : "n"(N) : "memory"); }
template <class T> DI void tie(T& a) { asm volatile("" : "+v"(a)); }
DI void wait_lds() { asm volatile("" ::: "memory"); __builtin_amdgcn_s_waitcnt(0xC07F); asm volatile("" ::: "memory"); }
DI void bar() { wait_lds(); asm volatile("s_barrier" ::: "memory"); }
DI bf16x8 lds_b128(unsigned a) { return *(LDSP bf16x8*)(smem + a); }
DI s16x4 lds_tr(unsigned a) { return __builtin_amdgcn_ds_read_tr16_b64_v4i16((LDSP s16x4*)(smem + a)); }
DI bf16x8 cat(s16x4 lo, s16x4 hi) { return __builtin_bit_cast(bf16x8, __builtin_shufflevector(lo, hi, 0, 1, 2, 3, 4, 5, 6, 7)); }
DI bf16x8 lds_tr2(unsigned a0, unsigned a1) { return cat(lds_tr(a0), lds_tr(a1)); }
DI bf16x8 asbf(u32x4 v) { return __builtin_bit_cast(bf16x8, v); }
DI f32x4 mfma32(bf16x8 a, bf16x8 b, f32x4 c) { return __builtin_amdgcn_mfma_f32_16x16x32_bf16(a, b, c, 0, 0, 0); }
DI f32x4 mfma16(u32x2 a, u32x2 b, f32x4 c) {
  return __builtin_amdgcn_mfma_f32_16x16x16bf16_1k(__builtin_bit_cast(s16x4, a), __builtin_bit_cast(s16x4, b), c, 0, 0, 0);
}
DI unsigned pk(float a, float b) { f32x2 f = {a, b}; return __builtin_bit_cast(unsigned, __builtin_convertvector(f, bf16x2)); }
DI float blo(unsigned v) { return __uint_as_float(v << 16); }
DI float bhi(unsigned v) { return __uint_as_float(v & 0xffff0000u); }
DI float dot2(unsigned a, unsigned b, float c) {
  return __builtin_amdgcn_fdot2_f32_bf16(__builtin_bit_cast(bf16x2, a), __builtin_bit_cast(bf16x2, b), c, false);
}
DI f32x2 swp16(float a, float b) {
  auto r = __builtin_amdgcn_permlane16_swap(__float_as_uint(a), __float_as_uint(b), false, false);
  f32x2 v = {__uint_as_float(r[0]), __uint_as_float(r[1])}; return v;
}
DI f32x2 swp32(float a, float b) {
  auto r = __builtin_amdgcn_permlane32_swap(__float_as_uint(a), __float_as_uint(b), false, false);
  f32x2 v = {__uint_as_float(r[0]), __uint_as_float(r[1])}; return v;
}
DI float sum2rows(float a, float b) { f32x2 p = swp16(a, b); float s = p[0] + p[1]; f32x2 q = swp32(s, s); return q[0] + q[1]; }
template <int CTRL> DI float dppf(float v) { return __int_as_float(__builtin_amdgcn_update_dpp(0, __float_as_int(v), CTRL, 0xf, 0xf, true)); }
DI u32x4 swap_pair(unsigned a0, unsigned a1, unsigned b0, unsigned b1) {
  auto r0 = __builtin_amdgcn_permlane16_swap(a0, b0, false, false);
  auto r1 = __builtin_amdgcn_permlane16_swap(a1, b1, false, false);
  u32x4 v = {r0[0], r1[0], r0[1], r1[1]};
  return v;
}
DI void publish(char* ws, unsigned off, u64 tag, int tid) {
  asm volatile("s_waitcnt vmcnt(0)" ::: "memory");
  bar();
  if (tid == 0) __hip_atomic_store((GLB u64*)(ws + off), tag, __ATOMIC_RELAXED, __HIP_MEMORY_SCOPE_AGENT);
}
DI void poll(char* ws, unsigned off, int n, u64 tag, int lane) {
  GLB u64* p = (GLB u64*)(ws + off) + (lane < n ? lane : 0);
#pragma unroll 1
  for (int spin = 0; spin < SPIN_MAX; ++spin) {
    u64 v = __hip_atomic_load(p, __ATOMIC_RELAXED, __HIP_MEMORY_SCOPE_AGENT);
    if (__all(v == tag)) break;
    __builtin_amdgcn_s_sleep(1);
  }
  asm volatile("" ::: "memory");
}

DI float fma1(float a, float b, float c) { float d = __builtin_fmaf(a, b, c); asm("" : "+v"(d)); return d; }
DI float mul1(float a, float b) { float d = a * b; asm("" : "+v"(d)); return d; }
#define MF "v_mfma_f32_16x16x32_bf16 "
DI void mstep(f32x4 (&c)[4], const bf16x8 (&wt)[4], u32x4 a, int acc) {
  if (acc) asm volatile(MF "%0, %4, %8, %0\n\t" MF "%1, %5, %8, %1\n\t" MF "%2, %6, %8, %2\n\t" MF "%3, %7, %8, %3"
                        : "+v"(c[0]), "+v"(c[1]), "+v"(c[2]), "+v"(c[3]) : "v"(wt[0]), "v"(wt[1]), "v"(wt[2]), "v"(wt[3]), "v"(a));
  else asm volatile(MF "%0, %4, %8, 0\n\t" MF "%1, %5, %8, 0\n\t" MF "%2, %6, %8, 0\n\t" MF "%3, %7, %8, 0"
                    : "=&v"(c[0]), "=&v"(c[1]), "=&v"(c[2]), "=&v"(c[3]) : "v"(wt[0]), "v"(wt[1]), "v"(wt[2]), "v"(wt[3]), "v"(a));
}
DI f32x16 mfmaL(bf16x8 a, bf16x8 b, f32x16 c) { return __builtin_amdgcn_mfma_f32_32x32x16_bf16(a, b, c, 0, 0, 0); }
DI f32x16 mfmaS(u32x2 a, u32x2 b, f32x16 c) {
  return __builtin_amdgcn_mfma_f32_32x32x8bf16_1k(__builtin_bit_cast(s16x4, a), __builtin_bit_cast(s16x4, b), c, 0, 0, 0);
}
template <int SC> DI void dmai(unsigned lb, unsigned lo, unsigned voff, rsrc_t r, unsigned sb, unsigned so) {
  unsigned t;
  if (SC) asm volatile("s_add_u32 m0, %1, %2\n\ts_add_u32 %0, %5, %6\n\tbuffer_load_dwordx4 %3, %4, %0 offen sc1 lds"
                       : "=&s"(t) : "s"(lb), "n"(lo), "v"(voff), "s"(r), "s"(sb), "n"(so) : "memory", "scc");
  else asm volatile("s_add_u32 m0, %1, %2\n\ts_add_u32 %0, %5, %6\n\tbuffer_load_dwordx4 %3, %4, %0 offen lds"
                    : "=&s"(t) : "s"(lb), "n"(lo), "v"(voff), "s"(r), "s"(sb), "n"(so) : "memory", "scc");
}
template <int V> struct IC { static constexpr int value = V; };
template <int A, int B, class F> DI void sfor(F&& f) { if constexpr (A < B) { f(IC<A>{}); sfor<A + 1, B>(f); } }

constexpr unsigned harea(int t) { return t == 3 ? 32768u : 65536u + 32768u * t; }
constexpr unsigned warea(int j) { return j >= 3 && j <= 5 ? harea(j - 3) : 0u; }
constexpr int NW = 6;
constexpr int oH(int t, int i) { return 16 + t * 4 + i; }
constexpr int oW(int j, int i) { return 32 + j * 4 + i; }
constexpr int oA(int t, int j) { return 64 + t * 8 + j; }
constexpr int oX(int i) { return 8 + i; }
constexpr int oS = 1, oF = 2, oP = 3;
constexpr int vm_at(int pos) {
  if (pos < 0) return pos == -1 ? oP : oW(0, pos + 5);
  const int t = pos / 33, n = pos % 33;
  if (t > 0 && n == 21) return oS;
  if (t == 0) {
    if (n % 3 == 1 && n <= 19) return oH(1, 1) + n / 3;
    if (n >= 23 && n % 3 == 2) return oW(1, (n - 23) / 3);
  } else if (t == 1) {
    if (n % 3 == 1 && n <= 10) return oH(3, n / 3);
    if (n % 3 == 1 && n <= 22) return oW(3, (n - 13) / 3);
    if (n >= 24 && n % 2 == 0 && n < 32) return oW(2, (n - 24) / 2);
  } else if (t == 2) {
    if (n % 3 == 1 && n <= 10) return oW(4, n / 3);
    if (n == 13) return oF;
    if (n >= 24 && n % 2 == 0 && n < 32) return oX((n - 24) / 2);
  } else if (t == 3) {
    if (n >= 1 && n <= 7 && (n & 1)) return oW(5, n / 2);
    if (n >= 10 && n <= 16 && !(n & 1)) return oW(6, (n - 10) / 2);
    if (n >= 18 && n != 21 && !(n & 1) && n <= 30) return oA(0, (n - 18) / 2);
  }
  return 0;
}
constexpr int younger(int op, int pos) {
  int c = -1;
  for (int p = -5; p < pos; ++p) { if (c >= 0 && vm_at(p)) ++c; if (vm_at(p) == op) c = 0; }
  return c;
}
constexpr int rd_at(int pos) {
  const int t = pos / 33, n = pos % 33;
  if (t == 0) return n == 14 ? 0 : n == 15 ? 1 : n == 17 ? 2 : n == 18 ? 3 : -1;
  if (t == 1) return n == 11 ? 4 : n == 12 ? 5 : n == 14 ? 6 : n == 15 ? 7 : -1;
  if (t == 3) return n >= 4 && n < 16 ? 4 + n : -1;
  return -1;
}

#define SPA 1
#define SPD 2
constexpr int pf_at(int s) { return s >= SPA && (s - SPA) % SPD == 0 && (s - SPA) / SPD < 4 ? (s - SPA) / SPD : -1; }
constexpr int first1(int s) { return s == 0 ? 15 : s == 1 ? 22 : s == 2 ? 29 : 5 * s + 20; }
constexpr int after1(int id, int at) {
  int c = id < 10 ? 9 - id + 1 + 5 : id < 15 ? 14 - id : -1;
  for (int s = 0; s < at; ++s) {
    if (s == 4) { if (c >= 0) ++c; if (id == 100) c = 0; }
    for (int p = first1(s); p < first1(s + 1); ++p)
      if (p < 40 || p % 5 != 4) { if (c >= 0) ++c; if (p == id) c = 0; }
    if (c >= 0) c += pf_at(s) >= 0;
  }
  return c;
}

__global__ __launch_bounds__(512, 2) void fused_block(const u16* __restrict__ x, const u16* __restrict__ wqkv,
                                                      const u16* __restrict__ wo, const u16* __restrict__ wg,
                                                      const u16* __restrict__ wu, const u16* __restrict__ wd,
                                                      u16* __restrict__ out, char* __restrict__ ws) {
  const int tid = threadIdx.x, lane = tid & 63;
  const unsigned w = rfl(tid >> 6);
  const unsigned l15 = lane & 15, g = lane >> 4, q = (lane >> 2) & 3, p4 = lane & 3;
  const unsigned b = blockIdx.x, xcd = b & 7, bi = b >> 3;
  const rsrc_t rWS = mk(ws, WS_END);
  TS(0);
  const unsigned rt = xcd * 4 + (bi & 3), cg = bi >> 2;
  const unsigned panel = (xcd >> 1) * 4 + (bi & 3), fg = (xcd & 1) * 7 + (bi >> 2);
  const unsigned off3 = bi < 28 ? 0u : 0x7f000000u;
  const unsigned prow = lane >> 3, pp = lane & 7;
  const unsigned fN = ((prow >> 1) & 1) | ((w & 1) << 1);
  const unsigned swzN = (((pp >> 1) ^ fN) << 5) | ((pp & 1) << 4);
  const unsigned frN = ((q >> 1) & 1) | ((g & 1) << 1);
  const unsigned trowN = (8 * g + q) * 128 + p4 * 8;
  const u32x4 ones = {0x3f803f80u, 0x3f803f80u, 0x3f803f80u, 0x3f803f80u};
  const rsrc_t rX = mk(x, S_ * D_ * 2);
  const unsigned vW3 = (8 * w + prow) * (F_ * 2) + fg * 256 + swzN + off3;
  const rsrc_t rG = mk(wg, D_ * F_ * 2), rU = mk(wu, D_ * F_ * 2);
  auto dma_q = [&](int qi, int i) {
    dma(((qi + 1) & 1) * 65536 + (i >> 1) * 16384 + (w + 8 * (i & 1)) * 1024, (i & 4) ? vW3 ^ 64 : vW3, (i & 4) ? rU : rG,
        (128 * qi + 64 * (i & 1)) * (F_ * 2) + ((i >> 1) & 1) * 128);
  };

  u64 tag, epoch;
  unsigned f1;
  {
    const unsigned rg = (xcd >> 1) * 8 + (bi & 7), hg = (xcd & 1) * 4 + (bi >> 3);
    const unsigned rot = ((bi & 7) + 2 * (bi >> 3)) & 7;
    const rsrc_t rW = mk(wqkv, D_ * 1536 * 2), rO = mk(wo, D_ * D_ * 2);
    u32x4 xrow;
    ld16(xrow, lane * 16, rX, (rg * 64 + hg * 8 + w) * 1024);
    const int r0 = 8 * w + prow;
    const unsigned vX0 = max((int)rg * 64 - 16 + r0, 0) * 1024 + ((pp ^ ((r0 >> 1) & 7)) << 4);
    const unsigned vX1 = (rg * 64 + 48 + 2 * w + (prow & 1)) * 1024 + ((pp ^ w) << 4);
    const unsigned vW = (8 * w + prow) * 3072 + hg * 128 + swzN;
    const unsigned vO = (8 * w + prow) * 1024 + cg * 128 + swzN;
    const u64 mX = w == 0 ? ~0xffull : ~0ull;
    auto piece = [&](int G, int i) {
      if (G < 8) {
        const unsigned st = (G + rot) & 7, sb = (G & 3) * SLOT;
        if (i == 0) dma_m(sb + w * 1024, vX0, rX, st * 128, mX);
        else if (i == 1) dma_m(sb + 8192 + w * 256, vX1, rX, st * 128, 0xffffull);
        else dma(sb + 10240 + (i - 2) * 8192 + w * 1024, vW, rW, st * 64 * 3072 + (i - 2) * 1024);
      } else if (i == 4) {
      } else if (G < 10) {
        dma((G - 8) * 32768 + (w + 8 * i) * 1024, vO, rO, (256 * (G - 8) + 64 * i) * 1024);
      } else {
        dma_q(0, 4 * (G - 10) + i);
      }
    };
#pragma unroll
    for (int i = 0; i < 5; ++i) piece(0, i);
    asm volatile("s_barrier" ::: "memory");
#pragma unroll
    for (int i = 0; i < 5; ++i) piece(1, i);
    TS(20);
    if (tid < 32) *(LDSP u32x4*)(smem + (tid >> 3) * SLOT + (tid & 7) * 16) = ones;
    epoch = *(const CST u64*)(ws + WS_EPOCH);
    tag = epoch * 0x9E3779B97F4A7C15ull + 0x632BE59BD9B4E019ull;
    const unsigned tag32 = (unsigned)(tag >> 32);
    {
      vm_tie<10>(xrow);
      TS(21);
      float s1 = 0.f, s2 = 0.f;
#pragma unroll
      for (int j = 0; j < 4; ++j) { s1 = dot2(xrow[j], 0x3f803f80u, s1); s2 = dot2(xrow[j], xrow[j], s2); }
      float s = sum2rows(s1, s2);
      s += dppf<0x128>(s); s += dppf<0x124>(s); s += dppf<0x122>(s); s += dppf<0x121>(s);
      const float m = __int_as_float(__builtin_amdgcn_readlane(__float_as_int(s), 0)) * (1.f / 512.f);
      const float e2 = __int_as_float(__builtin_amdgcn_readlane(__float_as_int(s), 16)) * (1.f / 512.f);
      const float rs = __builtin_amdgcn_rsqf(fmaxf(e2 - m * m, 0.f) + 1e-5f);
      const u32x4 gr = {__float_as_uint(m), tag32, __float_as_uint(rs), tag32};
      st16<16>(gr, rWS, lane == 0 ? 0u : 0x7ff00000u, WS_S1 + (rg * 64 + hg * 8 + w) * 16);
    }
    const unsigned hw = w & 3, half = w >> 2;
    const unsigned xa = (l15 * 128 + ((g ^ (l15 >> 1)) << 4) + half * 4096) ^ (half * 64);
    const unsigned wa = (10240 + trowN + ((hw ^ frN) << 5)) + half * 4096;
    const unsigned xb = xa ^ 64, wb = wa + 4096 - half * 8192;
    const unsigned par = l15 & 1;
    f32x4 mc, mp, mp1;
#pragma unroll
    for (int r = 0; r < 4; ++r) {
      const bool pm = (r & 1) == par, cur = 4 * g + r <= l15;
      mc[r] = (pm && cur) ? 0.f : -4e9f;
      mp[r] = (pm && !cur) ? 0.f : -4e9f;
      mp1[r] = (rg == 0 && half == 0) ? -4e9f : mp[r];
    }
    const unsigned se0 = par ? 0x01000c0cu : 0x0c0c0100u, se1 = par ? 0x03020c0cu : 0x0c0c0302u;
    const bool c0 = (4 * g + par) <= l15, c1 = (4 * g + 2 + par) <= l15;
    u32x4 sel = {c0 ? 0x0c0c0c0cu : se0, c1 ? 0x0c0c0c0cu : se1, c0 ? se0 : 0x0c0c0c0cu, c1 ? se1 : 0x0c0c0c0cu};
    unsigned vS = WS_S1 + max((int)(rg * 64 - 16 + half * 32 + (g < 3 ? g : 0) * 16 + l15), 0) * 16;
    tie(mc); tie(mp); tie(mp1); tie(sel); tie(vS);

    f32x4 aQ[2], aK[3], aV[3], aC = {0, 0, 0, 0};
#pragma unroll
    for (int i = 0; i < 3; ++i) { aK[i] = {0, 0, 0, 0}; aV[i] = {0, 0, 0, 0}; }
    aQ[0] = {0, 0, 0, 0}; aQ[1] = {0, 0, 0, 0};
    bf16x8 xf[2][2][3];
    s16x4 wl[2][2][3], wh[2][2][3];
    auto rd = [&](int s, int r) {
      const int kk = r / 9, i = r % 9;
      const unsigned sb = (s & 3) * SLOT;
      if (i < 3) xf[s & 1][kk][i] = lds_b128(sb + (kk ? xb : xa) + i * 2048);
      else {
        const int m = ((i - 3) / 2 + 1) % 3;
        const unsigned a = sb + (kk ? wb : wa) + m * 8192;
        if ((i - 3) & 1) wh[s & 1][kk][m] = lds_tr(a + 512); else wl[s & 1][kk][m] = lds_tr(a);
      }
    };
    auto mm = [&](int s, int n) {
      const int c = s & 1, kk = n > 8, i = kk ? n - 9 : n;
      const bf16x8 wq = cat(wl[c][kk][0], wh[c][kk][0]), wk = cat(wl[c][kk][1], wh[c][kk][1]), wv = cat(wl[c][kk][2], wh[c][kk][2]);
      SB();
      if (i < 3) aK[i] = mfma32(wk, xf[c][kk][i], aK[i]);
      else if (i < 6) aV[i - 3] = mfma32(xf[c][kk][i - 3], wv, aV[i - 3]);
      else if (i < 8) aQ[i - 6] = mfma32(wq, xf[c][kk][i - 5], aQ[i - 6]);
      else aC = mfma32(wq, asbf(ones), aC);
      SB();
    };
    TS(22);
    wait_vm<6>();
    TS(23);
    bar();
    TS(1);
#pragma unroll
    for (int r = 0; r < 18; ++r) rd(0, r);
    SB();
#pragma unroll
    for (int i = 0; i < 5; ++i) piece(2, i);
    u32x4 sl;
    const unsigned nP = hw == 3 ? 8 : 7, vP = lane < nP ? ((b * 2 + half) * nP + lane) * 128 : 0x7f000000u;
    SB();
    auto part = [&](int s, int n0, int n1) __attribute__((always_inline)) {
#pragma unroll
      for (int n = n0; n < n1; ++n) {
        mm(s, n);
        const int slot = n % 3 == 2 ? n / 3 + (n > 13) : n == 13 ? 4 : n == 16 ? 6 : -1;
        const int first = first1(s), cnt = first1(s + 1) - first;
        if (slot >= 0 && slot < cnt) piece((first + slot) / 5, (first + slot) % 5);
        if (n == 16 && pf_at(s) >= 0) touch(vP + pf_at(s) * (D_ * F_ / 2), mk(hw == 0 ? wg : hw == 1 ? wu : hw == 2 ? wd : wo, hw == 3 ? D_ * D_ * 2 : D_ * F_ * 2));
        if (n % 3 != 2 && n < 13 && s < 7) { rd(s + 1, 2 * (n - n / 3)); rd(s + 1, 2 * (n - n / 3) + 1); }
      }
    };
#pragma unroll
    for (int s = 0; s < 6; ++s) {
      if (s == 0) wait_vm<after1(9, 0)>(); else if (s == 1) wait_vm<after1(14, 1)>(); else if (s == 2) wait_vm<after1(19, 2)>();
      else if (s == 3) wait_vm<after1(24, 3)>(); else if (s == 4) wait_vm<after1(29, 4)>(); else wait_vm<after1(34, 5)>();
      bar();
      if (s == 4) { TS(2); ld16c<0>(sl, vS, rWS, 0); }
      part(s, 0, 17);
    }
    vm_tie<after1(100, 6)>(sl);
    bar();
    part(6, 0, 14);
#pragma unroll 1
    for (int spin = 0; spin < SPIN_MAX; ++spin) {
      if (__all(sl[1] == tag32 && sl[3] == tag32)) break;
      __builtin_amdgcn_s_sleep(1);
      ld16c<0>(sl, vS, rWS, 0);
      vm_tie<0>(sl);
    }
    *(LDSP unsigned*)(smem + L_FS + w * 512 + lane * 4) = sl[0];
    *(LDSP unsigned*)(smem + L_FS + w * 512 + 256 + lane * 4) = sl[2];
    part(6, 14, 17);
    bar();
    part(7, 0, 17);
    TS(3);
    *(LDSP f32x4*)(smem + L_CX + w * 2304 + lane * 16) = aC;
    *(LDSP f32x4*)(smem + L_CX + w * 2304 + 1024 + lane * 16) = aK[0];
    *(LDSP float*)(smem + L_CX + w * 2304 + 2048 + lane * 4) = aV[0][0];
    float sm[3], sr[3];
    f32x4 vm[3], vr[3];
#pragma unroll
    for (int t = 0; t < 3; ++t) {
      sm[t] = *(LDSP float*)(smem + L_FS + w * 512 + t * 64 + l15 * 4);
      sr[t] = *(LDSP float*)(smem + L_FS + w * 512 + 256 + t * 64 + l15 * 4);
      vm[t] = *(LDSP f32x4*)(smem + L_FS + w * 512 + t * 64 + g * 16);
      vr[t] = *(LDSP f32x4*)(smem + L_FS + w * 512 + 256 + t * 64 + g * 16);
    }
    wait_vm<after1(48, 8)>();
    bar();
    const f32x4 csq = aC + *(LDSP f32x4*)(smem + L_CX + (w ^ 4) * 2304 + lane * 16);
    const f32x4 csk = *(LDSP f32x4*)(smem + L_CX + hw * 2304 + 1024 + g * 256);
    const float csv = *(LDSP float*)(smem + L_CX + hw * 2304 + 2048 + l15 * 4);
    u32x2 qb[2], kb[3], vb[3];
#pragma unroll
    for (int t = 0; t < 3; ++t) {
      const f32x4 kv = (aK[t] - sm[t] * csk) * sr[t];
      kb[t] = {pk(kv[0], kv[1]), pk(kv[2], kv[3])};
      if (t > 0) { const f32x4 qv = (aQ[t - 1] - sm[t] * csq) * sr[t]; qb[t - 1] = {pk(qv[0], qv[1]), pk(qv[2], qv[3])}; }
      const f32x4 vv = (aV[t] - vm[t] * csv) * vr[t];
      vb[t] = {pk(vv[0], vv[1]), pk(vv[2], vv[3])};
    }
    float l0[2], l1[2];
#pragma unroll
    for (int t = 0; t < 2; ++t) {
      const f32x4 sc = mfma16(kb[t + 1], qb[t], mc), sp = mfma16(kb[t], qb[t], t ? mp : mp1);
      l0[t] = fmaxf(fmaxf(fmaxf(sc[0], sc[1]), sp[0]), sp[1]);
      l1[t] = fmaxf(fmaxf(fmaxf(sc[2], sc[3]), sp[2]), sp[3]);
    }
    f32x2 mx;
    {
      const f32x2 a = swp16(fmaxf(l0[0], l1[0]), fmaxf(l0[1], l1[1]));
      const float m1 = fmaxf(a[0], a[1]);
      const f32x2 c = swp32(m1, m1);
      const float m2 = fmaxf(c[0], c[1]) * (-0.25f * 1.44269504f);
      mx = swp16(m2, m2);
    }
    unsigned ob[2][2];
#pragma unroll
    for (int t = 0; t < 2; ++t) {
      const float e0 = __builtin_amdgcn_exp2f(fmaf(l0[t], 0.25f * 1.44269504f, mx[t])), e1 = __builtin_amdgcn_exp2f(fmaf(l1[t], 0.25f * 1.44269504f, mx[t]));
      const unsigned e = pk(e0, e1);
      u32x4 pf;
#pragma unroll
      for (int j = 0; j < 4; ++j) pf[j] = __builtin_amdgcn_perm(0u, e, sel[j]);
      const u32x4 vf = {vb[t][0], vb[t][1], vb[t + 1][0], vb[t + 1][1]};
      const f32x4 z = {0, 0, 0, 0};
      const f32x4 den = mfma32(asbf(ones), asbf(pf), z);
      const f32x4 o = mfma32(asbf(vf), asbf(pf), z) * __builtin_amdgcn_rcpf(den[0]);
      ob[t][0] = pk(o[0], o[1]); ob[t][1] = pk(o[2], o[3]);
    }
    {
      const u32x4 ov = swap_pair(ob[0][0], ob[0][1], ob[1][0], ob[1][1]);
      const unsigned head = hg * 4 + hw;
      const unsigned off = (g & 1) * 16384 + ((g >> 1) * 16 + l15) * 16;
      st16<16>(ov, rWS, off, WS_O + ((rg * 4 + half * 2) * 16 + (head >> 1)) * 1024 + (head & 1) * 512);
    }
    TS(4);
    f1 = WS_F1 + (rg * 16 + hg) * 8;
  }

  bf16x8 fw[32];
  const unsigned aq = ((g & 1) * 2 + (w >> 2)) * 16384 + (8 * (g >> 1) + q) * 128 + (((w & 3) ^ (q >> 1) ^ (g & 2) ^ ((g & 1) << 1)) << 5)
                      + (((p4 & 1) << 1) | (p4 >> 1)) * 8;
  auto rdq = [&](int qi) {
    unsigned a = aq + ((qi + 1) & 1) * 65536;
    tie(a);
#pragma unroll
    for (int ks = 0; ks < 8; ++ks) fw[8 * qi + ks] = lds_tr2(a + ks * 2048, a + ks * 2048 + 512);
    SB();
  };
  {
    const unsigned gp = ((g & 1) << 1) | (g >> 1);
    const unsigned m2 = (w >> 2) * 2 + (w & 1), kh = (w >> 1) & 1;
    u32x4 xr;
    ld16(xr, (m2 * 16 + l15) * 1024 + gp * 16, rX, rt * 65536 + cg * 128 + kh * 64);
    bf16x8 wf[8][4];
#pragma unroll
    for (int ks = 0; ks < 8; ++ks)
#pragma unroll
      for (int c = 0; c < 4; ++c) {
        const unsigned a = kh * 32768 + trowN + ((c ^ (2 * kh) ^ frN) << 5) + ks * 4096;
        wf[ks][c] = lds_tr2(a, a + 512);
      }
    SB();
    vm_tie<0>(xr);
    publish(ws, f1, tag, tid);
    TS(5);
    rdq(0);
#pragma unroll
    for (int i = 0; i < 8; ++i) dma_q(1, i);
    bar();
    TS(6);
    poll(ws, WS_F1 + (rt * 16 + kh * 4) * 8, 4, tag, lane);
    TS(7);
    u32x4 of[8];
    const unsigned ob = WS_O + ((rt * 4 + m2) * 16 + kh * 8) * 1024;
    ld16c<0>(of[0], lane * 16, rWS, ob); ld16c<1024>(of[1], lane * 16, rWS, ob); ld16c<2048>(of[2], lane * 16, rWS, ob); ld16c<3072>(of[3], lane * 16, rWS, ob);
    ld16c<0>(of[4], lane * 16, rWS, ob + 4096); ld16c<1024>(of[5], lane * 16, rWS, ob + 4096); ld16c<2048>(of[6], lane * 16, rWS, ob + 4096); ld16c<3072>(of[7], lane * 16, rWS, ob + 4096);
    f32x4 acc[4];
#pragma unroll
    for (int c = 0; c < 4; ++c) acc[c] = {0, 0, 0, 0};
#pragma unroll
    for (int ks = 0; ks < 8; ++ks) {
      vm_tie<7>(of[ks]);
#pragma unroll
      for (int c = 3; c >= 0; --c) acc[c] = mfma32(wf[ks][c], asbf(of[ks]), acc[c]);
      SB();
      dma_q(2, ks);
    }
    TS(8);
    *(LDSP f32x4*)(smem + L_RED + w * 2048 + lane * 16) = acc[2];
    *(LDSP f32x4*)(smem + L_RED + w * 2048 + 1024 + lane * 16) = acc[3];
    bar();
    const f32x4 k0 = acc[0] + *(LDSP f32x4*)(smem + L_RED + (w ^ 2) * 2048 + lane * 16);
    const f32x4 k1 = acc[1] + *(LDSP f32x4*)(smem + L_RED + (w ^ 2) * 2048 + 1024 + lane * 16);
    const u32x4 av = swap_pair(pk(k0[0], k0[1]), pk(k0[2], k0[3]), pk(k1[0], k1[1]), pk(k1[2], k1[3]));
    u32x4 hreg;
#pragma unroll
    for (int j = 0; j < 4; ++j) hreg[j] = pk(blo(av[j]) + blo(xr[j]), bhi(av[j]) + bhi(xr[j]));
    st16<16>(hreg, rWS, (gp * 16 + l15) * 16, WS_H + ((rt * 4 + m2) * 16 + cg * 2 + kh) * 1024);
    float s1 = 0.f, s2 = 0.f;
#pragma unroll
    for (int j = 0; j < 4; ++j) { s1 = dot2(hreg[j], 0x3f803f80u, s1); s2 = dot2(hreg[j], hreg[j], s2); }
    const float ss = sum2rows(s1, s2);
    __builtin_amdgcn_raw_buffer_store_b32(__float_as_uint(ss), rWS, g < 2 ? l15 * 8 + g * 4 : 0x7ff00000u, WS_S2 + ((rt * 4 + m2) * 16 + cg * 2 + kh) * 128, 16);
    SB();
    rdq(1);
    publish(ws, WS_F2 + (rt * 16 + cg) * 8, tag, tid);
    TS(9);
  }

  const rsrc_t rD = mk(wd, D_ * F_ * 2);
  const unsigned vD0 = prow * 1024 + cg * 128 + ((((pp >> 1) ^ ((prow >> 1) & 1)) << 5) | ((pp & 1) << 4)), vD1 = vD0 ^ 64;
  const unsigned lw = w * 1024, sD = w * (7 * 32 * 1024);
  auto dma_wd = [&](int j, int i, unsigned area) { dmai<0>(lw, area + i * 8192, (i & 1) ? vD1 : vD0, rD, sD, (j * 32 + i * 8) * 1024); };
  bf16x8 wdf[7][4];
  unsigned tw;
  auto mktw = [&](unsigned base) {
    unsigned ln = lane; tie(ln);
    tw = base + lw + (ln >> 4) * 8192 + ((ln >> 2) & 3) * 128 + (ln & 3) * 8 + ((((ln >> 3) & 1) | ((ln >> 3) & 2)) << 5);
    tie(tw);
  };
  auto rdw = [&](int j, int c, unsigned area) {
    unsigned a = tw ^ (c << 5);
    tie(a);
    wdf[j][c] = lds_tr2(a + (area & 65535), a + (area & 65535) + 512);
  };
  auto tiew = [&](int j) { asm volatile("" : "+v"(wdf[j][0]), "+v"(wdf[j][1]), "+v"(wdf[j][2]), "+v"(wdf[j][3])); };
  u32x2 fl;
  auto ldf = [&]() { unsigned ln = lane; tie(ln); ld8c<0>(fl, min(ln, 13u) * 8, rWS, WS_F3 + (rt >> 1) * 128); };
  const unsigned ab4 = WS_A + (rt * 4 * 56 + w * 7) * 1024;
  u32x4 af[3][7];
  auto lda = [&](int t, int j) {
    const unsigned so = ab4 + t * (56 * 1024) + (j >> 2) * 4096;
    u32x4& a = af[t % 3][j];
    if ((j & 3) == 0) ld16c<0>(a, lane * 16, rWS, so); else if ((j & 3) == 1) ld16c<1024>(a, lane * 16, rWS, so);
    else if ((j & 3) == 2) ld16c<2048>(a, lane * 16, rWS, so); else ld16c<3072>(a, lane * 16, rWS, so);
  };
  auto open = [&](const u32x4& a) { return __any(max(max(max(a[0], a[1]), a[2]), a[3]) == ~0u); };
  unsigned hv[4];
  auto ldx = [&](int t) {
    unsigned ln = lane; tie(ln);
    const unsigned fc = ln >> 4, fq = (ln >> 2) & 3, fr = (ln >> 1) & 1, fp = ln & 1;
    ld4c(hv[t], (fc >> 1) * 1024 + ((fc & 1) * 2 + (fq >> 1)) * 256 + fr * 16 + (fq & 1) * 8 + fp * 4, rWS, WS_H + ((rt * 4 + t) * 16 + cg * 2) * 1024 + w * 32);
  };
  auto phase4 = [&]() __attribute__((always_inline)) {
    f32x4 acc[2][4];
    auto chk = [&](int t, int j) {
      u32x4& a = af[t % 3][j];
      if (__builtin_expect(open(a), 0)) {
#pragma unroll 1
        for (int spin = 0; spin < SPIN_MAX; ++spin) {
          __builtin_amdgcn_s_sleep(2);
          lda(t, j);
          vm_tie<0>(a);
          if (!open(a)) break;
        }
      }
      SB();
    };
    auto mm = [&](int t, int j) { chk(t, j); mstep(acc[t & 1], wdf[j], af[t % 3][j], j); SB(); };
    constexpr int PA[4] = {3, 4, 5, 0};
    const unsigned pw0 = lw + (g * 16 + (l15 ^ (2 * g))) * 16, pw1 = pw0 ^ 128;
    auto put = [&](int t) {
      auto& a = acc[t & 1];
      asm volatile("s_nop 7" : "+v"(a[0]), "+v"(a[1]), "+v"(a[2]), "+v"(a[3]));
#pragma unroll
      for (int c = 0; c < 4; ++c) *(LDSP f32x4*)(smem + warea(PA[t]) + c * 8192 + ((c & 1) ? pw1 : pw0)) = acc[t & 1][c];
      SB();
    };
    const unsigned fc = lane >> 4, fq = (lane >> 2) & 3, fr = (lane >> 1) & 1, fp = lane & 1;
    const unsigned pr = fc * 8192 + (fq * 16 + ((2 * w + fr) ^ (2 * fq + 8 * (fc & 1)))) * 16 + fp * 8;
    const rsrc_t rOut = mk(out, S_ * D_ * 2);
    const unsigned vOut = fr * 1024 + fc * 32 + fq * 8 + fp * 4;
    auto fin = [&](int t) {
      f32x2 v[8];
#pragma unroll
      for (int s = 0; s < 8; ++s) v[s] = *(LDSP f32x2*)(smem + warea(PA[t]) + pr + s * 1024);
      const f32x2 r = ((v[0] + v[1]) + (v[2] + v[3])) + ((v[4] + v[5]) + (v[6] + v[7]));
      const unsigned f = pk(r[0], r[1]);
      __builtin_amdgcn_raw_buffer_store_b32(pk(blo(f) + blo(hv[t]), bhi(f) + bhi(hv[t])), rOut, vOut, (rt * 64 + t * 16 + 2 * w) * 1024 + cg * 128, 0);
    };
    TS(16);
#pragma unroll
    for (int j = 0; j < 7; ++j) { vm_tie<13>(af[0][j]); mm(0, j); lda(2, j); }
    TS(61);
    sfor<0, 7>([&](auto J) { constexpr int j = J.value; vm_tie<13 - j>(af[1][j]); mm(1, j); if (j == 0) put(0); });
    TS(62);
#pragma unroll
    for (int j = 0; j < 7; ++j) { vm_tie<6>(af[2][j]); mm(2, j); lda(3, j); if (j == 0) put(1); }
    put(2);
    TS(63);
    bar();
    fin(0); fin(1); fin(2);
    SB();
    sfor<0, 7>([&](auto J) { constexpr int j = J.value; vm_tie<9 - j>(af[0][j]); mm(3, j); });
    TS(17);
    put(3);
    bar();
    fin(3);
    TS(18);
  };
  {
    const unsigned r2 = (lane >> 4) & 1, hk = lane >> 5;
    unsigned ao = r2 * (4 * 56 * 1024) + hk * 256 + l15 * 16 + off3;
    const unsigned ab = WS_A + (panel * 8 * 56 + fg * 4 + (w >> 1)) * 1024 + (w & 1) * 512;
#pragma unroll
    for (int i = 0; i < 8; ++i) dma_q(3, i);
    {
      u32x4 none = {~0u, ~0u, ~0u, ~0u};
      tie(none);
#pragma unroll
      for (int t = 0; t < 4; ++t) st16<16>(none, rWS, ao, ab + t * (56 * 1024));
    }
    rdq(2);
    const unsigned s1 = ((l15 >> 2) & 1) == (g & 1) ? 0x3f803f80u : 0u;
    const u32x4 sel = {s1, s1, s1, s1};
    f32x4 cs = {0, 0, 0, 0};
#pragma unroll
    for (int ks = 0; ks < 24; ++ks) cs = mfma32(asbf(sel), fw[ks], cs);
    SB();
    wait_vm<4>();
    bar();
    TS(10);
    rdq(3);
#pragma unroll
    for (int ks = 24; ks < 32; ++ks) cs = mfma32(asbf(sel), fw[ks], cs);
    SB();
    bar();
    u32x2 ax;
    {
      const unsigned c1 = pk(cs[0], cs[0]);
      const float d = cs[0] - blo(c1);
      ax = {hk ? pk(d, d) : c1, 0u};
      tie(ax);
    }
    TS(14);
    const bool last = b == 255;
    poll(ws, (last ? WS_F1 + w * 512 : WS_F2 + panel * 256) + ((lane >> 3) & (last ? 3 : 1)) * 128, 8, tag, lane & 7);
    TS(11);
    u32x2 sp[4];
    {
      const unsigned so = WS_S2 + (panel * 8 + w) * 2048, vs = g * 512 + l15 * 8;
      ld8c<0>(sp[0], vs, rWS, so); ld8c<128>(sp[1], vs, rWS, so); ld8c<256>(sp[2], vs, rWS, so); ld8c<384>(sp[3], vs, rWS, so);
    }
    unsigned vH = r2 * 65536 + hk * 256 + l15 * 16 + off3;
    tie(vH); tie(ao);
    const unsigned hb = WS_H + panel * 8 * 16384 + w * 512;
    auto dma_h = [&](int t, int i) { dmai<1>(lw, harea(t) + i * 8192, vH, rWS, hb, t * 16384 + i * 4096); };
#pragma unroll
    for (int i = 0; i < 5; ++i) dma_h(i >> 2, i & 3);
#pragma unroll
    for (int i = 0; i < 4; ++i) dma_wd(0, i, 0);
    asm volatile("s_waitcnt vmcnt(9)" : "+v"(sp[0]), "+v"(sp[1]), "+v"(sp[2]), "+v"(sp[3]) :: "memory");
    {
      const f32x2 s = (__builtin_bit_cast(f32x2, sp[0]) + __builtin_bit_cast(f32x2, sp[1])) + (__builtin_bit_cast(f32x2, sp[2]) + __builtin_bit_cast(f32x2, sp[3]));
      const float r = sum2rows(s[0], s[1]) * (1.f / 512.f);
      const f32x2 me = swp16(r, r);
      const float var = fmaxf(me[1] - me[0] * me[0], 0.f) + 1e-5f;
      const float m1 = blo(pk(-me[0], 0.f));
      const u32x2 st = {pk(m1, -me[0] - m1), __float_as_uint(__builtin_amdgcn_rsqf(var) * -1.44269504f)};
      *(LDSP u32x2*)(smem + L_ST + ((w & 3) * 32 + (w >> 2) * 16 + l15) * 8) = st;
    }
    wait_vm<4>();
    bar();
    TS(12);
    unsigned ha[3] = {lane * 16u, lane * 16u + 65536, lane * 16u + 131072};
    bf16x8 hf[NW];
    auto rdh = [&](int f) { const unsigned a = harea(f >> 5) + (f & 31) * 1024; hf[f % NW] = lds_b128(ha[a >> 16] + (a & 65535)); };
    u32x2 st[4];
    tie(ha[1]);
#pragma unroll
    for (int t = 0; t < 4; ++t) st[t] = *(LDSP u32x2*)(smem + L_ST + t * 256 + (lane & 31) * 8);
#pragma unroll
    for (int f = 0; f < NW; ++f) rdh(f);
    SB();
    {
      const u64 fv = last ? epoch + 1 : tag;
      const u32x2 f2 = {(unsigned)fv, (unsigned)(fv >> 32)};
      __builtin_amdgcn_raw_buffer_store_b64(f2, rWS, tid ? 0x7ff00000u : last ? 0u : off3, last ? WS_EPOCH : WS_F3 + (panel * 16 + fg) * 8, 16);
    }
    SB();
    f32x16 acc[2];
    float kk;
    u32x4 av;
    auto epi = [&](int t, int k) {
      f32x16& a = acc[t & 1];
      const float sc = __uint_as_float(st[t][1]);
      if (k == 0) kk = __builtin_amdgcn_rcpf(sc);
      else if (k == 1) kk = mul1(kk, 1.44269504f * 1.44269504f);
      else {
        const int i = (k - 2) / 7, o = (k - 2) % 7;
        if (o == 0) { if (i < 8) a[i] = mul1(a[i], sc); }
        else if (o == 1) { if (i >= 1 && i < 9) a[i - 1] = fma1(a[i - 1], kk, kk); }
        else if (o == 2) { if (i < 8) a[8 + i] = mul1(a[i], a[8 + i]); }
        else if (o == 3) { if (i < 8) { float e = __builtin_amdgcn_exp2f(a[i]); tie(e); a[i] = e; } }
        else if (o == 4) { if (i >= 2) a[i - 2] = mul1(a[i - 2], a[8 + i - 2]); }
        else if (o == 5) { if (i >= 1 && i < 9) { float e = __builtin_amdgcn_rcpf(a[i - 1]); tie(e); a[i - 1] = e; } }
        else if (i >= 3 && (i & 1)) { unsigned v = min(pk(a[i - 3], a[i - 2]), ~1u); tie(v); av[(i - 3) >> 1] = v; }
      }
    };
    constexpr int NE = 72;
    sfor<0, 4 * 33>([&](auto P) __attribute__((always_inline)) {
      constexpr int pos = P.value, t = pos / 33, n = pos % 33, op = vm_at(pos), rd = rd_at(pos);
      if constexpr (n == 0) {
        const f32x16 z = {0, 0, 0, 0, 0, 0, 0, 0, 0, 0, 0, 0, 0, 0, 0, 0};
        const u32x2 bx = {st[t][0], st[t][0]};
        acc[t & 1] = mfmaS(ax, bx, z);
        tie(acc[t & 1]);
        SB();
        if constexpr (t > 0) {
          TS3(24 + 2 * t);
          wait_vm<younger(t == 3 ? oH(3, 3) : oH(t + 1, 0), pos)>();
          asm volatile("s_barrier" ::: "memory");
          TS3(25 + 2 * t);
        }
        if constexpr (t == 1) tie(ha[2]);
        if constexpr (t == 2) tie(ha[0]);
      } else {
        constexpr int f = t * 32 + n - 1;
        acc[t & 1] = mfmaL(fw[n - 1], hf[f % NW], acc[t & 1]);
        tie(acc[t & 1]);
        SB();
        if constexpr (f + NW < 128) rdh(f + NW);
      }
      SB();
      if constexpr (t > 0 && n >= 2 && n < 20) {
#pragma unroll
        for (int k = 4 * (n - 2); k < 4 * (n - 1); ++k) epi(t - 1, k);
      }
      SB();
      if constexpr (rd >= 0) {
        if constexpr ((rd & 3) == 0) { wait_vm<younger(oW(rd >> 2, 3), pos)>(); if (rd != 4 && rd != 16) mktw(warea(rd >> 2) & ~65535u); }
        rdw(rd >> 2, rd & 3, warea(rd >> 2));
      }
      if constexpr (op == oS) st16<16>(av, rWS, ao, ab + (t - 1) * (56 * 1024));
      else if constexpr (op == oF) ldf();
      else if constexpr (op >= oA(0, 0)) lda((op - 64) >> 3, op & 7);
      else if constexpr (op >= oW(0, 0)) {
        constexpr int j = (op - 32) >> 2;
        if constexpr ((op & 3) == 0 && (j == 1 || j == 2 || j == 6)) tiew(j == 6 ? 2 : j - 1);
        dma_wd(j, op & 3, warea(j));
      }
      else if constexpr (op >= oH(0, 0)) dma_h((op - 16) >> 2, op & 3);
      else if constexpr (op >= oX(0)) ldx(op - 8);
      if constexpr (t == 2 && n == 23) {
        vm_tie<younger(oF, pos + 1)>(fl);
        if (__builtin_expect(!__all(fl[0] == (unsigned)tag && fl[1] == (unsigned)(tag >> 32)), 0)) {
#pragma unroll 1
          for (int spin = 0; spin < SPIN_MAX; ++spin) {
            __builtin_amdgcn_s_sleep(1);
            ldf();
            vm_tie<0>(fl);
            if (__all(fl[0] == (unsigned)tag && fl[1] == (unsigned)(tag >> 32))) break;
          }
        }
      }
      SB();
    });
    TS(13);
    mktw(131072);
    asm volatile("s_waitcnt vmcnt(%4)" : "+v"(hv[0]), "+v"(hv[1]), "+v"(hv[2]), "+v"(hv[3]) : "n"(younger(oW(5, 3), 132)) : "memory");
#pragma unroll
    for (int c = 0; c < 4; ++c) rdw(5, c, warea(5));
    SB();
#pragma unroll
    for (int k = 0; k < NE; ++k) epi(3, k);
    st16<16>(av, rWS, ao, ab + 3 * (56 * 1024));
    SB();
    TS(15);
    mktw(0);
    wait_vm<younger(oW(6, 3), 132) + 1>();
#pragma unroll
    for (int j = 0; j < 7; ++j) { if (j < 4) rdw(6, j, 0); SB(); lda(1, j); }
    phase4();
  }
}

extern "C" void kernel_launch(void* const* d_in, const int* in_sizes, int n_in, void* d_out, int out_size, void* d_ws,
                              size_t ws_size, hipStream_t stream) {
  (void)hipFuncSetAttribute((const void*)fused_block, hipFuncAttributeMaxDynamicSharedMemorySize, LDS_BYTES);
  fused_block<<<256, 512, LDS_BYTES, stream>>>((const u16*)d_in[0], (const u16*)d_in[1], (const u16*)d_in[2], (const u16*)d_in[4],
                                               (const u16*)d_in[5], (const u16*)d_in[6], (u16*)d_out, (char*)d_ws);
}
```

```cpp
#define PADID 18
#define PADT 4500
#include <hip/hip_runtime.h>
#include <hip/hip_bf16.h>

typedef unsigned short u16;
typedef unsigned long long u64;
typedef __attribute__((ext_vector_type(8))) __bf16 bf16x8;
typedef __attribute__((ext_vector_type(2))) __bf16 bf16x2;
typedef __attribute__((ext_vector_type(4))) float f32x4;
typedef __attribute__((ext_vector_type(2))) float f32x2;
typedef __attribute__((ext_vector_type(16))) float f32x16;
typedef __attribute__((ext_vector_type(4))) short s16x4;
typedef __attribute__((ext_vector_type(8))) short s16x8;
typedef __attribute__((ext_vector_type(4))) unsigned u32x4;
typedef __attribute__((ext_vector_type(2))) unsigned u32x2;
#define LDSP __attribute__((address_space(3)))
#define GLB __attribute__((address_space(1)))
#define CST __attribute__((address_space(4)))
#define DI __device__ __forceinline__
#define SB() __builtin_amdgcn_sched_barrier(0)
typedef __amdgpu_buffer_rsrc_t rsrc_t;

constexpr int S_ = 2048, D_ = 512, F_ = 1792;
constexpr unsigned WS_EPOCH = 0;
constexpr unsigned WS_F1 = 4096;
constexpr unsigned WS_F2 = WS_F1 + 4096;
constexpr unsigned WS_S1 = 16384;
constexpr unsigned WS_F3 = WS_F2 + 4096;
constexpr unsigned WS_O = 131072;
constexpr unsigned WS_H = WS_O + 2097152;
constexpr unsigned WS_A = WS_H + 2097152;
constexpr unsigned WS_S2 = WS_A + 128 * 56 * 1024;
constexpr unsigned WS_END = WS_S2 + 128 * 2048;
constexpr unsigned SLOT = 34816;
constexpr unsigned L_SP = 4 * SLOT;
constexpr unsigned L_FS = L_SP;
constexpr unsigned L_CX = L_SP + 4096;
constexpr unsigned L_RED = L_SP + 4096;
constexpr unsigned L_ST = 32768;
constexpr unsigned LDS_BYTES = 163840;

extern __shared__ __attribute__((aligned(16))) char smem[];
#define TS(i)
#define TS3(i)
#define PRIO(x) do { SB(); __builtin_amdgcn_s_setprio(x); SB(); } while (0)

DI unsigned rfl(unsigned v) { return __builtin_amdgcn_readfirstlane(v); }
DI rsrc_t mk(const void* p, unsigned bytes) { return __builtin_amdgcn_make_buffer_rsrc((void*)p, (short)0, (int)bytes, 0x00020000); }
DI void dma(unsigned lds, unsigned voff, rsrc_t r, unsigned soff) {
  asm volatile("s_mov_b32 m0, %0\n\ts_nop 0\n\tbuffer_load_dwordx4 %1, %2, %3 offen lds"
               :: "s"(lds + (unsigned)(size_t)(LDSP char*)smem), "v"(voff), "s"(r), "s"(soff) : "memory");
}
DI void dma_m(unsigned lds, unsigned voff, rsrc_t r, unsigned soff, u64 mask) {
  asm volatile("s_mov_b32 m0, %0\n\ts_mov_b64 exec, %4\n\tbuffer_load_dwordx4 %1, %2, %3 offen lds\n\ts_mov_b64 exec, -1"
               :: "s"(lds + (unsigned)(size_t)(LDSP char*)smem), "v"(voff), "s"(r), "s"(soff), "s"(mask) : "memory");
}
DI void dmac(unsigned lds, unsigned voff, rsrc_t r, unsigned soff) {
  asm volatile("s_mov_b32 m0, %0\n\ts_nop 0\n\tbuffer_load_dwordx4 %1, %2, %3 offen sc1 lds"
               :: "s"(lds + (unsigned)(size_t)(LDSP char*)smem), "v"(voff), "s"(r), "s"(soff) : "memory");
}
template <int OFF> DI void ld16c(u32x4& d, unsigned voff, rsrc_t r, unsigned soff) {
  asm volatile("buffer_load_dwordx4 %0, %1, %2, %3 offen offset:%4 sc1" : "=v"(d) : "v"(voff), "s"(r), "s"(soff), "n"(OFF) : "memory");
}
template <int OFF> DI void ld8c(u32x2& d, unsigned voff, rsrc_t r, unsigned soff) {
  asm volatile("buffer_load_dwordx2 %0, %1, %2, %3 offen offset:%4 sc1" : "=v"(d) : "v"(voff), "s"(r), "s"(soff), "n"(OFF) : "memory");
}
DI void ld4c(unsigned& d, unsigned voff, rsrc_t r, unsigned soff) {
  asm volatile("buffer_load_dword %0, %1, %2, %3 offen sc1" : "=v"(d) : "v"(voff), "s"(r), "s"(soff) : "memory");
}
DI void ld16(u32x4& d, unsigned voff, rsrc_t r, unsigned soff) {
  asm volatile("buffer_load_dwordx4 %0, %1, %2, %3 offen" : "=v"(d) : "v"(voff), "s"(r), "s"(soff) : "memory");
}
DI void touch(unsigned voff, rsrc_t r) {
  asm volatile("buffer_load_dword v255, %0, %1, 0 offen" :: "v"(voff), "s"(r) : "memory", "v255");
}
template <int AUX> DI void st16(u32x4 v, rsrc_t r, unsigned voff, unsigned soff) {
  __builtin_amdgcn_raw_buffer_store_b128(v, r, voff, soff, AUX);
  asm volatile("s_nop 1" :: "v"(v) : "memory");
}
template <int N> DI void wait_vm() { asm volatile("s_waitcnt vmcnt(%0)" :: "n"(N) : "memory"); }
template <int N, class T> DI void vm_tie(T& a) { asm volatile("s_waitcnt vmcnt(%1)" : "+v"(a) : "n"(N) : "memory"); }
template <int N, class T> DI void vm_tie(T& a, T& b) { asm volatile("s_waitcnt vmcnt(%2)" : "+v"(a), "+v"(b) : "n"(N) : "memory"); }
template <class T> DI void tie(T& a) { asm volatile("" : "+v"(a)); }
DI void wait_lds() { asm volatile("" ::: "memory"); __builtin_amdgcn_s_waitcnt(0xC07F); asm volatile("" ::: "memory"); }
DI void bar() { wait_lds(); asm volatile("s_barrier" ::: "memory"); }
DI bf16x8 lds_b128(unsigned a) { return *(LDSP bf16x8*)(smem + a); }
DI s16x4 lds_tr(unsigned a) { return __builtin_amdgcn_ds_read_tr16_b64_v4i16((LDSP s16x4*)(smem + a)); }
DI bf16x8 cat(s16x4 lo, s16x4 hi) { return __builtin_bit_cast(bf16x8, __builtin_shufflevector(lo, hi, 0, 1, 2, 3, 4, 5, 6, 7)); }
DI bf16x8 lds_tr2(unsigned a0, unsigned a1) { return cat(lds_tr(a0), lds_tr(a1)); }
DI bf16x8 asbf(u32x4 v) { return __builtin_bit_cast(bf16x8, v); }
DI f32x4 mfma32(bf16x8 a, bf16x8 b, f32x4 c) { return __builtin_amdgcn_mfma_f32_16x16x32_bf16(a, b, c, 0, 0, 0); }
DI f32x4 mfma16(u32x2 a, u32x2 b, f32x4 c) {
  return __builtin_amdgcn_mfma_f32_16x16x16bf16_1k(__builtin_bit_cast(s16x4, a), __builtin_bit_cast(s16x4, b), c, 0, 0, 0);
}
DI unsigned pk(float a, float b) { f32x2 f = {a, b}; return __builtin_bit_cast(unsigned, __builtin_convertvector(f, bf16x2)); }
DI float blo(unsigned v) { return __uint_as_float(v << 16); }
DI float bhi(unsigned v) { return __uint_as_float(v & 0xffff0000u); }
DI float dot2(unsigned a, unsigned b, float c) {
  return __builtin_amdgcn_fdot2_f32_bf16(__builtin_bit_cast(bf16x2, a), __builtin_bit_cast(bf16x2, b), c, false);
}
DI f32x2 swp16(float a, float b) {
  auto r = __builtin_amdgcn_permlane16_swap(__float_as_uint(a), __float_as_uint(b), false, false);
  f32x2 v = {__uint_as_float(r[0]), __uint_as_float(r[1])}; return v;
}
DI f32x2 swp32(float a, float b) {
  auto r = __builtin_amdgcn_permlane32_swap(__float_as_uint(a), __float_as_uint(b), false, false);
  f32x2 v = {__uint_as_float(r[0]), __uint_as_float(r[1])}; return v;
}
DI float sum2rows(float a, float b) { f32x2 p = swp16(a, b); float s = p[0] + p[1]; f32x2 q = swp32(s, s); return q[0] + q[1]; }
template <int CTRL> DI float dppf(float v) { return __int_as_float(__builtin_amdgcn_update_dpp(0, __float_as_int(v), CTRL, 0xf, 0xf, true)); }
DI u32x4 swap_pair(unsigned a0, unsigned a1, unsigned b0, unsigned b1) {
  auto r0 = __builtin_amdgcn_permlane16_swap(a0, b0, false, false);
  auto r1 = __builtin_amdgcn_permlane16_swap(a1, b1, false, false);
  u32x4 v = {r0[0], r1[0], r0[1], r1[1]};
  return v;
}
DI void publish(char* ws, unsigned off, u64 tag, int tid) {
  asm volatile("s_waitcnt vmcnt(0)" ::: "memory");
  bar();
  if (tid == 0) __hip_atomic_store((GLB u64*)(ws + off), tag, __ATOMIC_RELAXED, __HIP_MEMORY_SCOPE_AGENT);
}
DI void poll(char* ws, unsigned off, int n, u64 tag, int lane) {
  GLB u64* p = (GLB u64*)(ws + off) + (lane < n ? lane : 0);
#pragma unroll 1
  for (int spin = 0; spin < SPIN_MAX; ++spin) {
    u64 v = __hip_atomic_load(p, __ATOMIC_RELAXED, __HIP_MEMORY_SCOPE_AGENT);
    if (__all(v == tag)) break;
    __builtin_amdgcn_s_sleep(1);
  }
  asm volatile("" ::: "memory");
}

DI float fma1(float a, float b, float c) { float d = __builtin_fmaf(a, b, c); asm("" : "+v"(d)); return d; }
DI float mul1(float a, float b) { float d = a * b; asm("" : "+v"(d)); return d; }
#define MF "v_mfma_f32_16x16x32_bf16 "
DI void mstep(f32x4 (&c)[4], const bf16x8 (&wt)[4], u32x4 a, int acc) {
  if (acc) asm volatile(MF "%0, %4, %8, %0\n\t" MF "%1, %5, %8, %1\n\t" MF "%2, %6, %8, %2\n\t" MF "%3, %7, %8, %3"
                        : "+v"(c[0]), "+v"(c[1]), "+v"(c[2]), "+v"(c[3]) : "v"(wt[0]), "v"(wt[1]), "v"(wt[2]), "v"(wt[3]), "v"(a));
  else asm volatile(MF "%0, %4, %8, 0\n\t" MF "%1, %5, %8, 0\n\t" MF "%2, %6, %8, 0\n\t" MF "%3, %7, %8, 0"
                    : "=&v"(c[0]), "=&v"(c[1]), "=&v"(c[2]), "=&v"(c[3]) : "v"(wt[0]), "v"(wt[1]), "v"(wt[2]), "v"(wt[3]), "v"(a));
}
DI f32x16 mfmaL(bf16x8 a, bf16x8 b, f32x16 c) { return __builtin_amdgcn_mfma_f32_32x32x16_bf16(a, b, c, 0, 0, 0); }
DI f32x16 mfmaS(u32x2 a, u32x2 b, f32x16 c) {
  return __builtin_amdgcn_mfma_f32_32x32x8bf16_1k(__builtin_bit_cast(s16x4, a), __builtin_bit_cast(s16x4, b), c, 0, 0, 0);
}
template <int SC> DI void dmai(unsigned lb, unsigned lo, unsigned voff, rsrc_t r, unsigned sb, unsigned so) {
  unsigned t;
  if (SC) asm volatile("s_add_u32 m0, %1, %2\n\ts_add_u32 %0, %5, %6\n\tbuffer_load_dwordx4 %3, %4, %0 offen sc1 lds"
                       : "=&s"(t) : "s"(lb), "n"(lo), "v"(voff), "s"(r), "s"(sb), "n"(so) : "memory", "scc");
  else asm volatile("s_add_u32 m0, %1, %2\n\ts_add_u32 %0, %5, %6\n\tbuffer_load_dwordx4 %3, %4, %0 offen lds"
                    : "=&s"(t) : "s"(lb), "n"(lo), "v"(voff), "s"(r), "s"(sb), "n"(so) : "memory", "scc");
}
template <int V> struct IC { static constexpr int value = V; };
template <int A, int B, class F> DI void sfor(F&& f) { if constexpr (A < B) { f(IC<A>{}); sfor<A + 1, B>(f); } }

constexpr unsigned harea(int t) { return t == 3 ? 32768u : 65536u + 32768u * t; }
constexpr unsigned warea(int j) { return j >= 3 && j <= 5 ? harea(j - 3) : 0u; }
constexpr int NW = 6;
constexpr int oH(int t, int i) { return 16 + t * 4 + i; }
constexpr int oW(int j, int i) { return 32 + j * 4 + i; }
constexpr int oA(int t, int j) { return 64 + t * 8 + j; }
constexpr int oX(int i) { return 8 + i; }
constexpr int oS = 1, oF = 2, oP = 3;
constexpr int vm_at(int pos) {
  if (pos < 0) return pos == -1 ? oP : oW(0, pos + 5);
  const int t = pos / 33, n = pos % 33;
  if (t > 0 && n == 21) return oS;
  if (t == 0) {
    if (n % 3 == 1 && n <= 19) return oH(1, 1) + n / 3;
    if (n >= 23 && n % 3 == 2) return oW(1, (n - 23) / 3);
  } else if (t == 1) {
    if (n % 3 == 1 && n <= 10) return oH(3, n / 3);
    if (n % 3 == 1 && n <= 22) return oW(3, (n - 13) / 3);
    if (n >= 24 && n % 2 == 0 && n < 32) return oW(2, (n - 24) / 2);
  } else if (t == 2) {
    if (n % 3 == 1 && n <= 10) return oW(4, n / 3);
    if (n == 13) return oF;
    if (n >= 24 && n % 2 == 0 && n < 32) return oX((n - 24) / 2);
  } else if (t == 3) {
    if (n >= 1 && n <= 7 && (n & 1)) return oW(5, n / 2);
    if (n >= 10 && n <= 16 && !(n & 1)) return oW(6, (n - 10) / 2);
    if (n >= 18 && n != 21 && !(n & 1) && n <= 30) return oA(0, (n - 18) / 2);
  }
  return 0;
}
constexpr int younger(int op, int pos) {
  int c = -1;
  for (int p = -5; p < pos; ++p) { if (c >= 0 && vm_at(p)) ++c; if (vm_at(p) == op) c = 0; }
  return c;
}
constexpr int rd_at(int pos) {
  const int t = pos / 33, n = pos % 33;
  if (t == 0) return n == 14 ? 0 : n == 15 ? 1 : n == 17 ? 2 : n == 18 ? 3 : -1;
  if (t == 1) return n == 11 ? 4 : n == 12 ? 5 : n == 14 ? 6 : n == 15 ? 7 : -1;
  if (t == 3) return n >= 4 && n < 16 ? 4 + n : -1;
  return -1;
}

#define SPA 1
#define SPD 2
constexpr int pf_at(int s) { return s >= SPA && (s - SPA) % SPD == 0 && (s - SPA) / SPD < 4 ? (s - SPA) / SPD : -1; }
constexpr int first1(int s) { return s == 0 ? 15 : s == 1 ? 22 : s == 2 ? 29 : 5 * s + 20; }
constexpr int after1(int id, int at) {
  int c = id < 10 ? 9 - id + 1 + 5 : id < 15 ? 14 - id : -1;
  for (int s = 0; s < at; ++s) {
    if (s == 4) { if (c >= 0) ++c; if (id == 100) c = 0; }
    for (int p = first1(s); p < first1(s + 1); ++p)
      if (p < 40 || p % 5 != 4) { if (c >= 0) ++c; if (p == id) c = 0; }
    if (c >= 0) c += pf_at(s) >= 0;
  }
  return c;
}

__global__ __launch_bounds__(512, 2) void fused_block(const u16* __restrict__ x, const u16* __restrict__ wqkv,
                                                      const u16* __restrict__ wo, const u16* __restrict__ wg,
                                                      const u16* __restrict__ wu, const u16* __restrict__ wd,
                                                      u16* __restrict__ out, char* __restrict__ ws) {
  const int tid = threadIdx.x, lane = tid & 63;
  const unsigned w = rfl(tid >> 6);
  const unsigned l15 = lane & 15, g = lane >> 4, q = (lane >> 2) & 3, p4 = lane & 3;
  const unsigned b = blockIdx.x, xcd = b & 7, bi = b >> 3;
  const rsrc_t rWS = mk(ws, WS_END);
  TS(0);
  const u64 pt0 = __builtin_amdgcn_s_memrealtime();
#define PAD(i) do { if (PADID == i) { SB(); while (__builtin_amdgcn_s_memrealtime() - pt0 < PADT) __builtin_amdgcn_s_sleep(1); SB(); } } while (0)
  const unsigned rt = xcd * 4 + (bi & 3), cg = bi >> 2;
  const unsigned panel = (xcd >> 1) * 4 + (bi & 3), fg = (xcd & 1) * 7 + (bi >> 2);
  const unsigned off3 = bi < 28 ? 0u : 0x7f000000u;
  const unsigned prow = lane >> 3, pp = lane & 7;
  const unsigned fN = ((prow >> 1) & 1) | ((w & 1) << 1);
  const unsigned swzN = (((pp >> 1) ^ fN) << 5) | ((pp & 1) << 4);
  const unsigned frN = ((q >> 1) & 1) | ((g & 1) << 1);
  const unsigned trowN = (8 * g + q) * 128 + p4 * 8;
  const u32x4 ones = {0x3f803f80u, 0x3f803f80u, 0x3f803f80u, 0x3f803f80u};
  const rsrc_t rX = mk(x, S_ * D_ * 2);
  const unsigned vW3 = (8 * w + prow) * (F_ * 2) + fg * 256 + swzN + off3;
  const rsrc_t rG = mk(wg, D_ * F_ * 2), rU = mk(wu, D_ * F_ * 2);
  auto dma_q = [&](int qi, int i) {
    dma(((qi + 1) & 1) * 65536 + (i >> 1) * 16384 + (w + 8 * (i & 1)) * 1024, (i & 4) ? vW3 ^ 64 : vW3, (i & 4) ? rU : rG,
        (128 * qi + 64 * (i & 1)) * (F_ * 2) + ((i >> 1) & 1) * 128);
  };

  u64 tag, epoch;
  unsigned f1;
  {
    const unsigned rg = (xcd >> 1) * 8 + (bi & 7), hg = (xcd & 1) * 4 + (bi >> 3);
    const unsigned rot = ((bi & 7) + 2 * (bi >> 3)) & 7;
    const rsrc_t rW = mk(wqkv, D_ * 1536 * 2), rO = mk(wo, D_ * D_ * 2);
    u32x4 xrow;
    ld16(xrow, lane * 16, rX, (rg * 64 + hg * 8 + w) * 1024);
    const int r0 = 8 * w + prow;
    const unsigned vX0 = max((int)rg * 64 - 16 + r0, 0) * 1024 + ((pp ^ ((r0 >> 1) & 7)) << 4);
    const unsigned vX1 = (rg * 64 + 48 + 2 * w + (prow & 1)) * 1024 + ((pp ^ w) << 4);
    const unsigned vW = (8 * w + prow) * 3072 + hg * 128 + swzN;
    const unsigned vO = (8 * w + prow) * 1024 + cg * 128 + swzN;
    const u64 mX = w == 0 ? ~0xffull : ~0ull;
    auto piece = [&](int G, int i) {
      if (G < 8) {
        const unsigned st = (G + rot) & 7, sb = (G & 3) * SLOT;
        if (i == 0) dma_m(sb + w * 1024, vX0, rX, st * 128, mX);
        else if (i == 1) dma_m(sb + 8192 + w * 256, vX1, rX, st * 128, 0xffffull);
        else dma(sb + 10240 + (i - 2) * 8192 + w * 1024, vW, rW, st * 64 * 3072 + (i - 2) * 1024);
      } else if (i == 4) {
      } else if (G < 10) {
        dma((G - 8) * 32768 + (w + 8 * i) * 1024, vO, rO, (256 * (G - 8) + 64 * i) * 1024);
      } else {
        dma_q(0, 4 * (G - 10) + i);
      }
    };
#pragma unroll
    for (int i = 0; i < 5; ++i) piece(0, i);
    asm volatile("s_barrier" ::: "memory");
#pragma unroll
    for (int i = 0; i < 5; ++i) piece(1, i);
    TS(20);
    if (tid < 32) *(LDSP u32x4*)(smem + (tid >> 3) * SLOT + (tid & 7) * 16) = ones;
    epoch = *(const CST u64*)(ws + WS_EPOCH);
    tag = epoch * 0x9E3779B97F4A7C15ull + 0x632BE59BD9B4E019ull;
    const unsigned tag32 = (unsigned)(tag >> 32);
    {
      vm_tie<10>(xrow);
      TS(21);
      float s1 = 0.f, s2 = 0.f;
#pragma unroll
      for (int j = 0; j < 4; ++j) { s1 = dot2(xrow[j], 0x3f803f80u, s1); s2 = dot2(xrow[j], xrow[j], s2); }
      float s = sum2rows(s1, s2);
      s += dppf<0x128>(s); s += dppf<0x124>(s); s += dppf<0x122>(s); s += dppf<0x121>(s);
      const float m = __int_as_float(__builtin_amdgcn_readlane(__float_as_int(s), 0)) * (1.f / 512.f);
      const float e2 = __int_as_float(__builtin_amdgcn_readlane(__float_as_int(s), 16)) * (1.f / 512.f);
      const float rs = __builtin_amdgcn_rsqf(fmaxf(e2 - m * m, 0.f) + 1e-5f);
      const u32x4 gr = {__float_as_uint(m), tag32, __float_as_uint(rs), tag32};
      st16<16>(gr, rWS, lane == 0 ? 0u : 0x7ff00000u, WS_S1 + (rg * 64 + hg * 8 + w) * 16);
    }
    const unsigned hw = w & 3, half = w >> 2;
    const unsigned xa = (l15 * 128 + ((g ^ (l15 >> 1)) << 4) + half * 4096) ^ (half * 64);
    const unsigned wa = (10240 + trowN + ((hw ^ frN) << 5)) + half * 4096;
    const unsigned xb = xa ^ 64, wb = wa + 4096 - half * 8192;
    const unsigned par = l15 & 1;
    f32x4 mc, mp, mp1;
#pragma unroll
    for (int r = 0; r < 4; ++r) {
      const bool pm = (r & 1) == par, cur = 4 * g + r <= l15;
      mc[r] = (pm && cur) ? 0.f : -4e9f;
      mp[r] = (pm && !cur) ? 0.f : -4e9f;
      mp1[r] = (rg == 0 && half == 0) ? -4e9f : mp[r];
    }
    const unsigned se0 = par ? 0x01000c0cu : 0x0c0c0100u, se1 = par ? 0x03020c0cu : 0x0c0c0302u;
    const bool c0 = (4 * g + par) <= l15, c1 = (4 * g + 2 + par) <= l15;
    u32x4 sel = {c0 ? 0x0c0c0c0cu : se0, c1 ? 0x0c0c0c0cu : se1, c0 ? se0 : 0x0c0c0c0cu, c1 ? se1 : 0x0c0c0c0cu};
    unsigned vS = WS_S1 + max((int)(rg * 64 - 16 + half * 32 + (g < 3 ? g : 0) * 16 + l15), 0) * 16;
    tie(mc); tie(mp); tie(mp1); tie(sel); tie(vS);

    f32x4 aQ[2], aK[3], aV[3], aC = {0, 0, 0, 0};
#pragma unroll
    for (int i = 0; i < 3; ++i) { aK[i] = {0, 0, 0, 0}; aV[i] = {0, 0, 0, 0}; }
    aQ[0] = {0, 0, 0, 0}; aQ[1] = {0, 0, 0, 0};
    bf16x8 xf[2][2][3];
    s16x4 wl[2][2][3], wh[2][2][3];
    auto rd = [&](int s, int r) {
      const int kk = r / 9, i = r % 9;
      const unsigned sb = (s & 3) * SLOT;
      if (i < 3) xf[s & 1][kk][i] = lds_b128(sb + (kk ? xb : xa) + i * 2048);
      else {
        const int m = ((i - 3) / 2 + 1) % 3;
        const unsigned a = sb + (kk ? wb : wa) + m * 8192;
        if ((i - 3) & 1) wh[s & 1][kk][m] = lds_tr(a + 512); else wl[s & 1][kk][m] = lds_tr(a);
      }
    };
    auto mm = [&](int s, int n) {
      const int c = s & 1, kk = n > 8, i = kk ? n - 9 : n;
      const bf16x8 wq = cat(wl[c][kk][0], wh[c][kk][0]), wk = cat(wl[c][kk][1], wh[c][kk][1]), wv = cat(wl[c][kk][2], wh[c][kk][2]);
      SB();
      if (i < 3) aK[i] = mfma32(wk, xf[c][kk][i], aK[i]);
      else if (i < 6) aV[i - 3] = mfma32(xf[c][kk][i - 3], wv, aV[i - 3]);
      else if (i < 8) aQ[i - 6] = mfma32(wq, xf[c][kk][i - 5], aQ[i - 6]);
      else aC = mfma32(wq, asbf(ones), aC);
      SB();
    };
    TS(22);
    wait_vm<6>();
    TS(23);
    bar();
    TS(1);
    PAD(1);
#pragma unroll
    for (int r = 0; r < 18; ++r) rd(0, r);
    SB();
#pragma unroll
    for (int i = 0; i < 5; ++i) piece(2, i);
    u32x4 sl;
    const unsigned nP = hw == 3 ? 8 : 7, vP = lane < nP ? ((b * 2 + half) * nP + lane) * 128 : 0x7f000000u;
    SB();
    auto part = [&](int s, int n0, int n1) __attribute__((always_inline)) {
#pragma unroll
      for (int n = n0; n < n1; ++n) {
        mm(s, n);
        const int slot = n % 3 == 2 ? n / 3 + (n > 13) : n == 13 ? 4 : n == 16 ? 6 : -1;
        const int first = first1(s), cnt = first1(s + 1) - first;
        if (slot >= 0 && slot < cnt) piece((first + slot) / 5, (first + slot) % 5);
        if (n == 16 && pf_at(s) >= 0) touch(vP + pf_at(s) * (D_ * F_ / 2), mk(hw == 0 ? wg : hw == 1 ? wu : hw == 2 ? wd : wo, hw == 3 ? D_ * D_ * 2 : D_ * F_ * 2));
        if (n % 3 != 2 && n < 13 && s < 7) { rd(s + 1, 2 * (n - n / 3)); rd(s + 1, 2 * (n - n / 3) + 1); }
      }
    };
#pragma unroll
    for (int s = 0; s < 6; ++s) {
      if (s == 0) wait_vm<after1(9, 0)>(); else if (s == 1) wait_vm<after1(14, 1)>(); else if (s == 2) wait_vm<after1(19, 2)>();
      else if (s == 3) wait_vm<after1(24, 3)>(); else if (s == 4) wait_vm<after1(29, 4)>(); else wait_vm<after1(34, 5)>();
      bar();
      if (s == 4) { TS(2); ld16c<0>(sl, vS, rWS, 0); }
      part(s, 0, 17);
    }
    vm_tie<after1(100, 6)>(sl);
    bar();
    part(6, 0, 14);
#pragma unroll 1
    for (int spin = 0; spin < SPIN_MAX; ++spin) {
      if (__all(sl[1] == tag32 && sl[3] == tag32)) break;
      __builtin_amdgcn_s_sleep(1);
      ld16c<0>(sl, vS, rWS, 0);
      vm_tie<0>(sl);
    }
    *(LDSP unsigned*)(smem + L_FS + w * 512 + lane * 4) = sl[0];
    *(LDSP unsigned*)(smem + L_FS + w * 512 + 256 + lane * 4) = sl[2];
    part(6, 14, 17);
    bar();
    part(7, 0, 17);
    TS(3);
    *(LDSP f32x4*)(smem + L_CX + w * 2304 + lane * 16) = aC;
    *(LDSP f32x4*)(smem + L_CX + w * 2304 + 1024 + lane * 16) = aK[0];
    *(LDSP float*)(smem + L_CX + w * 2304 + 2048 + lane * 4) = aV[0][0];
    float sm[3], sr[3];
    f32x4 vm[3], vr[3];
#pragma unroll
    for (int t = 0; t < 3; ++t) {
      sm[t] = *(LDSP float*)(smem + L_FS + w * 512 + t * 64 + l15 * 4);
      sr[t] = *(LDSP float*)(smem + L_FS + w * 512 + 256 + t * 64 + l15 * 4);
      vm[t] = *(LDSP f32x4*)(smem + L_FS + w * 512 + t * 64 + g * 16);
      vr[t] = *(LDSP f32x4*)(smem + L_FS + w * 512 + 256 + t * 64 + g * 16);
    }
    wait_vm<after1(48, 8)>();
    bar();
    const f32x4 csq = aC + *(LDSP f32x4*)(smem + L_CX + (w ^ 4) * 2304 + lane * 16);
    const f32x4 csk = *(LDSP f32x4*)(smem + L_CX + hw * 2304 + 1024 + g * 256);
    const float csv = *(LDSP float*)(smem + L_CX + hw * 2304 + 2048 + l15 * 4);
    u32x2 qb[2], kb[3], vb[3];
#pragma unroll
    for (int t = 0; t < 3; ++t) {
      const f32x4 kv = (aK[t] - sm[t] * csk) * sr[t];
      kb[t] = {pk(kv[0], kv[1]), pk(kv[2], kv[3])};
      if (t > 0) { const f32x4 qv = (aQ[t - 1] - sm[t] * csq) * sr[t]; qb[t - 1] = {pk(qv[0], qv[1]), pk(qv[2], qv[3])}; }
      const f32x4 vv = (aV[t] - vm[t] * csv) * vr[t];
      vb[t] = {pk(vv[0], vv[1]), pk(vv[2], vv[3])};
    }
    float l0[2], l1[2];
#pragma unroll
    for (int t = 0; t < 2; ++t) {
      const f32x4 sc = mfma16(kb[t + 1], qb[t], mc), sp = mfma16(kb[t], qb[t], t ? mp : mp1);
      l0[t] = fmaxf(fmaxf(fmaxf(sc[0], sc[1]), sp[0]), sp[1]);
      l1[t] = fmaxf(fmaxf(fmaxf(sc[2], sc[3]), sp[2]), sp[3]);
    }
    f32x2 mx;
    {
      const f32x2 a = swp16(fmaxf(l0[0], l1[0]), fmaxf(l0[1], l1[1]));
      const float m1 = fmaxf(a[0], a[1]);
      const f32x2 c = swp32(m1, m1);
      const float m2 = fmaxf(c[0], c[1]) * (-0.25f * 1.44269504f);
      mx = swp16(m2, m2);
    }
    unsigned ob[2][2];
#pragma unroll
    for (int t = 0; t < 2; ++t) {
      const float e0 = __builtin_amdgcn_exp2f(fmaf(l0[t], 0.25f * 1.44269504f, mx[t])), e1 = __builtin_amdgcn_exp2f(fmaf(l1[t], 0.25f * 1.44269504f, mx[t]));
      const unsigned e = pk(e0, e1);
      u32x4 pf;
#pragma unroll
      for (int j = 0; j < 4; ++j) pf[j] = __builtin_amdgcn_perm(0u, e, sel[j]);
      const u32x4 vf = {vb[t][0], vb[t][1], vb[t + 1][0], vb[t + 1][1]};
      const f32x4 z = {0, 0, 0, 0};
      const f32x4 den = mfma32(asbf(ones), asbf(pf), z);
      const f32x4 o = mfma32(asbf(vf), asbf(pf), z) * __builtin_amdgcn_rcpf(den[0]);
      ob[t][0] = pk(o[0], o[1]); ob[t][1] = pk(o[2], o[3]);
    }
    {
      const u32x4 ov = swap_pair(ob[0][0], ob[0][1], ob[1][0], ob[1][1]);
      const unsigned head = hg * 4 + hw;
      const unsigned off = (g & 1) * 16384 + ((g >> 1) * 16 + l15) * 16;
      st16<16>(ov, rWS, off, WS_O + ((rg * 4 + half * 2) * 16 + (head >> 1)) * 1024 + (head & 1) * 512);
    }
    TS(4);
    f1 = WS_F1 + (rg * 16 + hg) * 8;
  }

  bf16x8 fw[32];
  const unsigned aq = ((g & 1) * 2 + (w >> 2)) * 16384 + (8 * (g >> 1) + q) * 128 + (((w & 3) ^ (q >> 1) ^ (g & 2) ^ ((g & 1) << 1)) << 5)
                      + (((p4 & 1) << 1) | (p4 >> 1)) * 8;
  auto rdq = [&](int qi) {
    unsigned a = aq + ((qi + 1) & 1) * 65536;
    tie(a);
#pragma unroll
    for (int ks = 0; ks < 8; ++ks) fw[8 * qi + ks] = lds_tr2(a + ks * 2048, a + ks * 2048 + 512);
    SB();
  };
  {
    const unsigned gp = ((g & 1) << 1) | (g >> 1);
    const unsigned m2 = (w >> 2) * 2 + (w & 1), kh = (w >> 1) & 1;
    u32x4 xr;
    ld16(xr, (m2 * 16 + l15) * 1024 + gp * 16, rX, rt * 65536 + cg * 128 + kh * 64);
    bf16x8 wf[8][4];
#pragma unroll
    for (int ks = 0; ks < 8; ++ks)
#pragma unroll
      for (int c = 0; c < 4; ++c) {
        const unsigned a = kh * 32768 + trowN + ((c ^ (2 * kh) ^ frN) << 5) + ks * 4096;
        wf[ks][c] = lds_tr2(a, a + 512);
      }
    SB();
    vm_tie<0>(xr);
    publish(ws, f1, tag, tid);
    TS(5);
    PAD(5);
    rdq(0);
#pragma unroll
    for (int i = 0; i < 8; ++i) dma_q(1, i);
    bar();
    TS(6);
    poll(ws, WS_F1 + (rt * 16 + kh * 4) * 8, 4, tag, lane);
    TS(7);
    u32x4 of[8];
    const unsigned ob = WS_O + ((rt * 4 + m2) * 16 + kh * 8) * 1024;
    ld16c<0>(of[0], lane * 16, rWS, ob); ld16c<1024>(of[1], lane * 16, rWS, ob); ld16c<2048>(of[2], lane * 16, rWS, ob); ld16c<3072>(of[3], lane * 16, rWS, ob);
    ld16c<0>(of[4], lane * 16, rWS, ob + 4096); ld16c<1024>(of[5], lane * 16, rWS, ob + 4096); ld16c<2048>(of[6], lane * 16, rWS, ob + 4096); ld16c<3072>(of[7], lane * 16, rWS, ob + 4096);
    f32x4 acc[4];
#pragma unroll
    for (int c = 0; c < 4; ++c) acc[c] = {0, 0, 0, 0};
#pragma unroll
    for (int ks = 0; ks < 8; ++ks) {
      vm_tie<7>(of[ks]);
#pragma unroll
      for (int c = 3; c >= 0; --c) acc[c] = mfma32(wf[ks][c], asbf(of[ks]), acc[c]);
      SB();
      dma_q(2, ks);
    }
    TS(8);
    *(LDSP f32x4*)(smem + L_RED + w * 2048 + lane * 16) = acc[2];
    *(LDSP f32x4*)(smem + L_RED + w * 2048 + 1024 + lane * 16) = acc[3];
    bar();
    const f32x4 k0 = acc[0] + *(LDSP f32x4*)(smem + L_RED + (w ^ 2) * 2048 + lane * 16);
    const f32x4 k1 = acc[1] + *(LDSP f32x4*)(smem + L_RED + (w ^ 2) * 2048 + 1024 + lane * 16);
    const u32x4 av = swap_pair(pk(k0[0], k0[1]), pk(k0[2], k0[3]), pk(k1[0], k1[1]), pk(k1[2], k1[3]));
    u32x4 hreg;
#pragma unroll
    for (int j = 0; j < 4; ++j) hreg[j] = pk(blo(av[j]) + blo(xr[j]), bhi(av[j]) + bhi(xr[j]));
    st16<16>(hreg, rWS, (gp * 16 + l15) * 16, WS_H + ((rt * 4 + m2) * 16 + cg * 2 + kh) * 1024);
    float s1 = 0.f, s2 = 0.f;
#pragma unroll
    for (int j = 0; j < 4; ++j) { s1 = dot2(hreg[j], 0x3f803f80u, s1); s2 = dot2(hreg[j], hreg[j], s2); }
    const float ss = sum2rows(s1, s2);
    __builtin_amdgcn_raw_buffer_store_b32(__float_as_uint(ss), rWS, g < 2 ? l15 * 8 + g * 4 : 0x7ff00000u, WS_S2 + ((rt * 4 + m2) * 16 + cg * 2 + kh) * 128, 16);
    SB();
    rdq(1);
    publish(ws, WS_F2 + (rt * 16 + cg) * 8, tag, tid);
    TS(9);
  }

  const rsrc_t rD = mk(wd, D_ * F_ * 2);
  const unsigned vD0 = prow * 1024 + cg * 128 + ((((pp >> 1) ^ ((prow >> 1) & 1)) << 5) | ((pp & 1) << 4)), vD1 = vD0 ^ 64;
  const unsigned lw = w * 1024, sD = w * (7 * 32 * 1024);
  auto dma_wd = [&](int j, int i, unsigned area) { dmai<0>(lw, area + i * 8192, (i & 1) ? vD1 : vD0, rD, sD, (j * 32 + i * 8) * 1024); };
  bf16x8 wdf[7][4];
  unsigned tw;
  auto mktw = [&](unsigned base) {
    unsigned ln = lane; tie(ln);
    tw = base + lw + (ln >> 4) * 8192 + ((ln >> 2) & 3) * 128 + (ln & 3) * 8 + ((((ln >> 3) & 1) | ((ln >> 3) & 2)) << 5);
    tie(tw);
  };
  auto rdw = [&](int j, int c, unsigned area) {
    unsigned a = tw ^ (c << 5);
    tie(a);
    wdf[j][c] = lds_tr2(a + (area & 65535), a + (area & 65535) + 512);
  };
  auto tiew = [&](int j) { asm volatile("" : "+v"(wdf[j][0]), "+v"(wdf[j][1]), "+v"(wdf[j][2]), "+v"(wdf[j][3])); };
  u32x2 fl;
  auto ldf = [&]() { unsigned ln = lane; tie(ln); ld8c<0>(fl, min(ln, 13u) * 8, rWS, WS_F3 + (rt >> 1) * 128); };
  const unsigned ab4 = WS_A + (rt * 4 * 56 + w * 7) * 1024;
  u32x4 af[3][7];
  auto lda = [&](int t, int j) {
    const unsigned so = ab4 + t * (56 * 1024) + (j >> 2) * 4096;
    u32x4& a = af[t % 3][j];
    if ((j & 3) == 0) ld16c<0>(a, lane * 16, rWS, so); else if ((j & 3) == 1) ld16c<1024>(a, lane * 16, rWS, so);
    else if ((j & 3) == 2) ld16c<2048>(a, lane * 16, rWS, so); else ld16c<3072>(a, lane * 16, rWS, so);
  };
  auto open = [&](const u32x4& a) { return __any(max(max(max(a[0], a[1]), a[2]), a[3]) == ~0u); };
  unsigned hv[4];
  auto ldx = [&](int t) {
    unsigned ln = lane; tie(ln);
    const unsigned fc = ln >> 4, fq = (ln >> 2) & 3, fr = (ln >> 1) & 1, fp = ln & 1;
    ld4c(hv[t], (fc >> 1) * 1024 + ((fc & 1) * 2 + (fq >> 1)) * 256 + fr * 16 + (fq & 1) * 8 + fp * 4, rWS, WS_H + ((rt * 4 + t) * 16 + cg * 2) * 1024 + w * 32);
  };
  auto phase4 = [&]() __attribute__((always_inline)) {
    f32x4 acc[2][4];
    auto chk = [&](int t, int j) {
      u32x4& a = af[t % 3][j];
      if (__builtin_expect(open(a), 0)) {
#pragma unroll 1
        for (int spin = 0; spin < SPIN_MAX; ++spin) {
          __builtin_amdgcn_s_sleep(2);
          lda(t, j);
          vm_tie<0>(a);
          if (!open(a)) break;
        }
      }
      SB();
    };
    auto mm = [&](int t, int j) { chk(t, j); mstep(acc[t & 1], wdf[j], af[t % 3][j], j); SB(); };
    constexpr int PA[4] = {3, 4, 5, 0};
    const unsigned pw0 = lw + (g * 16 + (l15 ^ (2 * g))) * 16, pw1 = pw0 ^ 128;
    auto put = [&](int t) {
      auto& a = acc[t & 1];
      asm volatile("s_nop 7" : "+v"(a[0]), "+v"(a[1]), "+v"(a[2]), "+v"(a[3]));
#pragma unroll
      for (int c = 0; c < 4; ++c) *(LDSP f32x4*)(smem + warea(PA[t]) + c * 8192 + ((c & 1) ? pw1 : pw0)) = acc[t & 1][c];
      SB();
    };
    const unsigned fc = lane >> 4, fq = (lane >> 2) & 3, fr = (lane >> 1) & 1, fp = lane & 1;
    const unsigned pr = fc * 8192 + (fq * 16 + ((2 * w + fr) ^ (2 * fq + 8 * (fc & 1)))) * 16 + fp * 8;
    const rsrc_t rOut = mk(out, S_ * D_ * 2);
    const unsigned vOut = fr * 1024 + fc * 32 + fq * 8 + fp * 4;
    auto fin = [&](int t) {
      f32x2 v[8];
#pragma unroll
      for (int s = 0; s < 8; ++s) v[s] = *(LDSP f32x2*)(smem + warea(PA[t]) + pr + s * 1024);
      const f32x2 r = ((v[0] + v[1]) + (v[2] + v[3])) + ((v[4] + v[5]) + (v[6] + v[7]));
      const unsigned f = pk(r[0], r[1]);
      __builtin_amdgcn_raw_buffer_store_b32(pk(blo(f) + blo(hv[t]), bhi(f) + bhi(hv[t])), rOut, vOut, (rt * 64 + t * 16 + 2 * w) * 1024 + cg * 128, 0);
    };
    TS(16);
#pragma unroll
    for (int j = 0; j < 7; ++j) { vm_tie<13>(af[0][j]); mm(0, j); lda(2, j); }
    TS(61);
    sfor<0, 7>([&](auto J) { constexpr int j = J.value; vm_tie<13 - j>(af[1][j]); mm(1, j); if (j == 0) put(0); });
    TS(62);
#pragma unroll
    for (int j = 0; j < 7; ++j) { vm_tie<6>(af[2][j]); mm(2, j); lda(3, j); if (j == 0) put(1); }
    put(2);
    TS(63);
    bar();
    fin(0); fin(1); fin(2);
    SB();
    sfor<0, 7>([&](auto J) { constexpr int j = J.value; vm_tie<9 - j>(af[0][j]); mm(3, j); });
    TS(17);
    put(3);
    bar();
    fin(3);
    TS(18);
    PAD(18);
  };
  {
    const unsigned r2 = (lane >> 4) & 1, hk = lane >> 5;
    unsigned ao = r2 * (4 * 56 * 1024) + hk * 256 + l15 * 16 + off3;
    const unsigned ab = WS_A + (panel * 8 * 56 + fg * 4 + (w >> 1)) * 1024 + (w & 1) * 512;
#pragma unroll
    for (int i = 0; i < 8; ++i) dma_q(3, i);
    {
      u32x4 none = {~0u, ~0u, ~0u, ~0u};
      tie(none);
#pragma unroll
      for (int t = 0; t < 4; ++t) st16<16>(none, rWS, ao, ab + t * (56 * 1024));
    }
    rdq(2);
    const unsigned s1 = ((l15 >> 2) & 1) == (g & 1) ? 0x3f803f80u : 0u;
    const u32x4 sel = {s1, s1, s1, s1};
    f32x4 cs = {0, 0, 0, 0};
#pragma unroll
    for (int ks = 0; ks < 24; ++ks) cs = mfma32(asbf(sel), fw[ks], cs);
    SB();
    wait_vm<4>();
    bar();
    TS(10);
    rdq(3);
#pragma unroll
    for (int ks = 24; ks < 32; ++ks) cs = mfma32(asbf(sel), fw[ks], cs);
    SB();
    bar();
    u32x2 ax;
    {
      const unsigned c1 = pk(cs[0], cs[0]);
      const float d = cs[0] - blo(c1);
      ax = {hk ? pk(d, d) : c1, 0u};
      tie(ax);
    }
    TS(14);
    const bool last = b == 255;
    poll(ws, (last ? WS_F1 + w * 512 : WS_F2 + panel * 256) + ((lane >> 3) & (last ? 3 : 1)) * 128, 8, tag, lane & 7);
    TS(11);
    u32x2 sp[4];
    {
      const unsigned so = WS_S2 + (panel * 8 + w) * 2048, vs = g * 512 + l15 * 8;
      ld8c<0>(sp[0], vs, rWS, so); ld8c<128>(sp[1], vs, rWS, so); ld8c<256>(sp[2], vs, rWS, so); ld8c<384>(sp[3], vs, rWS, so);
    }
    unsigned vH = r2 * 65536 + hk * 256 + l15 * 16 + off3;
    tie(vH); tie(ao);
    const unsigned hb = WS_H + panel * 8 * 16384 + w * 512;
    auto dma_h = [&](int t, int i) { dmai<1>(lw, harea(t) + i * 8192, vH, rWS, hb, t * 16384 + i * 4096); };
#pragma unroll
    for (int i = 0; i < 5; ++i) dma_h(i >> 2, i & 3);
#pragma unroll
    for (int i = 0; i < 4; ++i) dma_wd(0, i, 0);
    asm volatile("s_waitcnt vmcnt(9)" : "+v"(sp[0]), "+v"(sp[1]), "+v"(sp[2]), "+v"(sp[3]) :: "memory");
    {
      const f32x2 s = (__builtin_bit_cast(f32x2, sp[0]) + __builtin_bit_cast(f32x2, sp[1])) + (__builtin_bit_cast(f32x2, sp[2]) + __builtin_bit_cast(f32x2, sp[3]));
      const float r = sum2rows(s[0], s[1]) * (1.f / 512.f);
      const f32x2 me = swp16(r, r);
      const float var = fmaxf(me[1] - me[0] * me[0], 0.f) + 1e-5f;
      const float m1 = blo(pk(-me[0], 0.f));
      const u32x2 st = {pk(m1, -me[0] - m1), __float_as_uint(__builtin_amdgcn_rsqf(var) * -1.44269504f)};
      *(LDSP u32x2*)(smem + L_ST + ((w & 3) * 32 + (w >> 2) * 16 + l15) * 8) = st;
    }
    wait_vm<4>();
    bar();
    TS(12);
    PAD(12);
    unsigned ha[3] = {lane * 16u, lane * 16u + 65536, lane * 16u + 131072};
    bf16x8 hf[NW];
    auto rdh = [&](int f) { const unsigned a = harea(f >> 5) + (f & 31) * 1024; hf[f % NW] = lds_b128(ha[a >> 16] + (a & 65535)); };
    u32x2 st[4];
    tie(ha[1]);
#pragma unroll
    for (int t = 0; t < 4; ++t) st[t] = *(LDSP u32x2*)(smem + L_ST + t * 256 + (lane & 31) * 8);
#pragma unroll
    for (int f = 0; f < NW; ++f) rdh(f);
    SB();
    {
      const u64 fv = last ? epoch + 1 : tag;
      const u32x2 f2 = {(unsigned)fv, (unsigned)(fv >> 32)};
      __builtin_amdgcn_raw_buffer_store_b64(f2, rWS, tid ? 0x7ff00000u : last ? 0u : off3, last ? WS_EPOCH : WS_F3 + (panel * 16 + fg) * 8, 16);
    }
    SB();
    f32x16 acc[2];
    float kk;
    u32x4 av;
    auto epi = [&](int t, int k) {
      f32x16& a = acc[t & 1];
      const float sc = __uint_as_float(st[t][1]);
      if (k == 0) kk = __builtin_amdgcn_rcpf(sc);
      else if (k == 1) kk = mul1(kk, 1.44269504f * 1.44269504f);
      else {
        const int i = (k - 2) / 7, o = (k - 2) % 7;
        if (o == 0) { if (i < 8) a[i] = mul1(a[i], sc); }
        else if (o == 1) { if (i >= 1 && i < 9) a[i - 1] = fma1(a[i - 1], kk, kk); }
        else if (o == 2) { if (i < 8) a[8 + i] = mul1(a[i], a[8 + i]); }
        else if (o == 3) { if (i < 8) { float e = __builtin_amdgcn_exp2f(a[i]); tie(e); a[i] = e; } }
        else if (o == 4) { if (i >= 2) a[i - 2] = mul1(a[i - 2], a[8 + i - 2]); }
        else if (o == 5) { if (i >= 1 && i < 9) { float e = __builtin_amdgcn_rcpf(a[i - 1]); tie(e); a[i - 1] = e; } }
        else if (i >= 3 && (i & 1)) { unsigned v = min(pk(a[i - 3], a[i - 2]), ~1u); tie(v); av[(i - 3) >> 1] = v; }
      }
    };
    constexpr int NE = 72;
    sfor<0, 4 * 33>([&](auto P) __attribute__((always_inline)) {
      constexpr int pos = P.value, t = pos / 33, n = pos % 33, op = vm_at(pos), rd = rd_at(pos);
      if constexpr (n == 0) {
        const f32x16 z = {0, 0, 0, 0, 0, 0, 0, 0, 0, 0, 0, 0, 0, 0, 0, 0};
        const u32x2 bx = {st[t][0], st[t][0]};
        acc[t & 1] = mfmaS(ax, bx, z);
        tie(acc[t & 1]);
        SB();
        if constexpr (t > 0) {
          TS3(24 + 2 * t);
          wait_vm<younger(t == 3 ? oH(3, 3) : oH(t + 1, 0), pos)>();
          asm volatile("s_barrier" ::: "memory");
          TS3(25 + 2 * t);
        }
        if constexpr (t == 1) tie(ha[2]);
        if constexpr (t == 2) tie(ha[0]);
      } else {
        constexpr int f = t * 32 + n - 1;
        acc[t & 1] = mfmaL(fw[n - 1], hf[f % NW], acc[t & 1]);
        tie(acc[t & 1]);
        SB();
        if constexpr (f + NW < 128) rdh(f + NW);
      }
      SB();
      if constexpr (t > 0 && n >= 2 && n < 20) {
#pragma unroll
        for (int k = 4 * (n - 2); k < 4 * (n - 1); ++k) epi(t - 1, k);
      }
      SB();
      if constexpr (rd >= 0) {
        if constexpr ((rd & 3) == 0) { wait_vm<younger(oW(rd >> 2, 3), pos)>(); if (rd != 4 && rd != 16) mktw(warea(rd >> 2) & ~65535u); }
        rdw(rd >> 2, rd & 3, warea(rd >> 2));
      }
      if constexpr (op == oS) st16<16>(av, rWS, ao, ab + (t - 1) * (56 * 1024));
      else if constexpr (op == oF) ldf();
      else if constexpr (op >= oA(0, 0)) lda((op - 64) >> 3, op & 7);
      else if constexpr (op >= oW(0, 0)) {
        constexpr int j = (op - 32) >> 2;
        if constexpr ((op & 3) == 0 && (j == 1 || j == 2 || j == 6)) tiew(j == 6 ? 2 : j - 1);
        dma_wd(j, op & 3, warea(j));
      }
      else if constexpr (op >= oH(0, 0)) dma_h((op - 16) >> 2, op & 3);
      else if constexpr (op >= oX(0)) ldx(op - 8);
      if constexpr (t == 2 && n == 23) {
        vm_tie<younger(oF, pos + 1)>(fl);
        if (__builtin_expect(!__all(fl[0] == (unsigned)tag && fl[1] == (unsigned)(tag >> 32)), 0)) {
#pragma unroll 1
          for (int spin = 0; spin < SPIN_MAX; ++spin) {
            __builtin_amdgcn_s_sleep(1);
            ldf();
            vm_tie<0>(fl);
            if (__all(fl[0] == (unsigned)tag && fl[1] == (unsigned)(tag >> 32))) break;
          }
        }
      }
      SB();
    });
    TS(13);
    PAD(13);
    mktw(131072);
    asm volatile("s_waitcnt vmcnt(%4)" : "+v"(hv[0]), "+v"(hv[1]), "+v"(hv[2]), "+v"(hv[3]) : "n"(younger(oW(5, 3), 132)) : "memory");
#pragma unroll
    for (int c = 0; c < 4; ++c) rdw(5, c, warea(5));
    SB();
#pragma unroll
    for (int k = 0; k < NE; ++k) epi(3, k);
    st16<16>(av, rWS, ao, ab + 3 * (56 * 1024));
    SB();
    TS(15);
    mktw(0);
    wait_vm<younger(oW(6, 3), 132) + 1>();
#pragma unroll
    for (int j = 0; j < 7; ++j) { if (j < 4) rdw(6, j, 0); SB(); lda(1, j); }
    phase4();
  }
}

extern "C" void kernel_launch(void* const* d_in, const int* in_sizes, int n_in, void* d_out, int out_size, void* d_ws,
                              size_t ws_size, hipStream_t stream) {
  (void)hipFuncSetAttribute((const void*)fused_block, hipFuncAttributeMaxDynamicSharedMemorySize, LDS_BYTES);
  fused_block<<<256, 512, LDS_BYTES, stream>>>((const u16*)d_in[0], (const u16*)d_in[1], (const u16*)d_in[2], (const u16*)d_in[4],
                                               (const u16*)d_in[5], (const u16*)d_in[6], (u16*)d_out, (char*)d_ws);
}
```

```cpp
#include <hip/hip_runtime.h>
#include <hip/hip_bf16.h>

typedef unsigned short u16;
typedef unsigned long long u64;
typedef __attribute__((ext_vector_type(8))) __bf16 bf16x8;
typedef __attribute__((ext_vector_type(2))) __bf16 bf16x2;
typedef __attribute__((ext_vector_type(4))) float f32x4;
typedef __attribute__((ext_vector_type(2))) float f32x2;
typedef __attribute__((ext_vector_type(16))) float f32x16;
typedef __attribute__((ext_vector_type(4))) short s16x4;
typedef __attribute__((ext_vector_type(8))) short s16x8;
typedef __attribute__((ext_vector_type(4))) unsigned u32x4;
typedef __attribute__((ext_vector_type(2))) unsigned u32x2;
#define LDSP __attribute__((address_space(3)))
#define GLB __attribute__((address_space(1)))
#define CST __attribute__((address_space(4)))
#define DI __device__ __forceinline__
#define SB() __builtin_amdgcn_sched_barrier(0)
typedef __amdgpu_buffer_rsrc_t rsrc_t;

constexpr int S_ = 2048, D_ = 512, F_ = 1792;
constexpr unsigned WS_EPOCH = 0;
constexpr unsigned WS_F1 = 4096;
constexpr unsigned WS_F2 = WS_F1 + 4096;
constexpr unsigned WS_S1 = 16384;
constexpr unsigned WS_F3 = WS_F2 + 4096;
constexpr unsigned WS_O = 131072;
constexpr unsigned WS_H = WS_O + 2097152;
constexpr unsigned WS_A = WS_H + 2097152;
constexpr unsigned WS_S2 = WS_A + 128 * 56 * 1024;
constexpr unsigned WS_END = WS_S2 + 128 * 2048;
constexpr unsigned SLOT = 34816;
constexpr unsigned L_SP = 4 * SLOT;
constexpr unsigned L_FS = L_SP;
constexpr unsigned L_CX = L_SP + 4096;
constexpr unsigned L_RED = L_SP + 4096;
constexpr unsigned L_ST = 32768;
constexpr unsigned LDS_BYTES = 163840;

extern __shared__ __attribute__((aligned(16))) char smem[];
#define TS(i)
#define TS3(i)
#define PRIO(x) do { SB(); __builtin_amdgcn_s_setprio(x); SB(); } while (0)

DI unsigned rfl(unsigned v) { return __builtin_amdgcn_readfirstlane(v); }
DI rsrc_t mk(const void* p, unsigned bytes) { return __builtin_amdgcn_make_buffer_rsrc((void*)p, (short)0, (int)bytes, 0x00020000); }
DI void dma(unsigned lds, unsigned voff, rsrc_t r, unsigned soff) {
  asm volatile("s_mov_b32 m0, %0\n\ts_nop 0\n\tbuffer_load_dwordx4 %1, %2, %3 offen lds"
               :: "s"(lds + (unsigned)(size_t)(LDSP char*)smem), "v"(voff), "s"(r), "s"(soff) : "memory");
}
DI void dma_m(unsigned lds, unsigned voff, rsrc_t r, unsigned soff, u64 mask) {
  asm volatile("s_mov_b32 m0, %0\n\ts_mov_b64 exec, %4\n\tbuffer_load_dwordx4 %1, %2, %3 offen lds\n\ts_mov_b64 exec, -1"
               :: "s"(lds + (unsigned)(size_t)(LDSP char*)smem), "v"(voff), "s"(r), "s"(soff), "s"(mask) : "memory");
}
DI void dmac(unsigned lds, unsigned voff, rsrc_t r, unsigned soff) {
  asm volatile("s_mov_b32 m0, %0\n\ts_nop 0\n\tbuffer_load_dwordx4 %1, %2, %3 offen sc1 lds"
               :: "s"(lds + (unsigned)(size_t)(LDSP char*)smem), "v"(voff), "s"(r), "s"(soff) : "memory");
}
template <int OFF> DI void ld16c(u32x4& d, unsigned voff, rsrc_t r, unsigned soff) {
  asm volatile("buffer_load_dwordx4 %0, %1, %2, %3 offen offset:%4 sc1" : "=v"(d) : "v"(voff), "s"(r), "s"(soff), "n"(OFF) : "memory");
}
template <int OFF> DI void ld8c(u32x2& d, unsigned voff, rsrc_t r, unsigned soff) {
  asm volatile("buffer_load_dwordx2 %0, %1, %2, %3 offen offset:%4 sc1" : "=v"(d) : "v"(voff), "s"(r), "s"(soff), "n"(OFF) : "memory");
}
DI void ld4c(unsigned& d, unsigned voff, rsrc_t r, unsigned soff) {
  asm volatile("buffer_load_dword %0, %1, %2, %3 offen sc1" : "=v"(d) : "v"(voff), "s"(r), "s"(soff) : "memory");
}
DI void ld16(u32x4& d, unsigned voff, rsrc_t r, unsigned soff) {
  asm volatile("buffer_load_dwordx4 %0, %1, %2, %3 offen" : "=v"(d) : "v"(voff), "s"(r), "s"(soff) : "memory");
}
DI void touch(unsigned voff, rsrc_t r) {
  asm volatile("buffer_load_dword v255, %0, %1, 0 offen" :: "v"(voff), "s"(r) : "memory", "v255");
}
template <int AUX> DI void st16(u32x4 v, rsrc_t r, unsigned voff, unsigned soff) {
  __builtin_amdgcn_raw_buffer_store_b128(v, r, voff, soff, AUX);
  asm volatile("s_nop 1" :: "v"(v) : "memory");
}
template <int N> DI void wait_vm() { asm volatile("s_waitcnt vmcnt(%0)" :: "n"(N) : "memory"); }
template <int N, class T> DI void vm_tie(T& a) { asm volatile("s_waitcnt vmcnt(%1)" : "+v"(a) : "n"(N) : "memory"); }
template <int N, class T> DI void vm_tie(T& a, T& b) { asm volatile("s_waitcnt vmcnt(%2)" : "+v"(a), "+v"(b) : "n"(N) : "memory"); }
template <class T> DI void tie(T& a) { asm volatile("" : "+v"(a)); }
DI void wait_lds() { asm volatile("" ::: "memory"); __builtin_amdgcn_s_waitcnt(0xC07F); asm volatile("" ::: "memory"); }
DI void bar() { wait_lds(); asm volatile("s_barrier" ::: "memory"); }
DI bf16x8 lds_b128(unsigned a) { return *(LDSP bf16x8*)(smem + a); }
DI s16x4 lds_tr(unsigned a) { return __builtin_amdgcn_ds_read_tr16_b64_v4i16((LDSP s16x4*)(smem + a)); }
DI bf16x8 cat(s16x4 lo, s16x4 hi) { return __builtin_bit_cast(bf16x8, __builtin_shufflevector(lo, hi, 0, 1, 2, 3, 4, 5, 6, 7)); }
DI bf16x8 lds_tr2(unsigned a0, unsigned a1) { return cat(lds_tr(a0), lds_tr(a1)); }
DI bf16x8 asbf(u32x4 v) { return __builtin_bit_cast(bf16x8, v); }
DI f32x4 mfma32(bf16x8 a, bf16x8 b, f32x4 c) { return __builtin_amdgcn_mfma_f32_16x16x32_bf16(a, b, c, 0, 0, 0); }
DI f32x4 mfma16(u32x2 a, u32x2 b, f32x4 c) {
  return __builtin_amdgcn_mfma_f32_16x16x16bf16_1k(__builtin_bit_cast(s16x4, a), __builtin_bit_cast(s16x4, b), c, 0, 0, 0);
}
DI unsigned pk(float a, float b) { f32x2 f = {a, b}; return __builtin_bit_cast(unsigned, __builtin_convertvector(f, bf16x2)); }
DI float blo(unsigned v) { return __uint_as_float(v << 16); }
DI float bhi(unsigned v) { return __uint_as_float(v & 0xffff0000u); }
DI float dot2(unsigned a, unsigned b, float c) {
  return __builtin_amdgcn_fdot2_f32_bf16(__builtin_bit_cast(bf16x2, a), __builtin_bit_cast(bf16x2, b), c, false);
}
DI f32x2 swp16(float a, float b) {
  auto r = __builtin_amdgcn_permlane16_swap(__float_as_uint(a), __float_as_uint(b), false, false);
  f32x2 v = {__uint_as_float(r[0]), __uint_as_float(r[1])}; return v;
}
DI f32x2 swp32(float a, float b) {
  auto r = __builtin_amdgcn_permlane32_swap(__float_as_uint(a), __float_as_uint(b), false, false);
  f32x2 v = {__uint_as_float(r[0]), __uint_as_float(r[1])}; return v;
}
DI float sum2rows(float a, float b) { f32x2 p = swp16(a, b); float s = p[0] + p[1]; f32x2 q = swp32(s, s); return q[0] + q[1]; }
template <int CTRL> DI float dppf(float v) { return __int_as_float(__builtin_amdgcn_update_dpp(0, __float_as_int(v), CTRL, 0xf, 0xf, true)); }
DI u32x4 swap_pair(unsigned a0, unsigned a1, unsigned b0, unsigned b1) {
  auto r0 = __builtin_amdgcn_permlane16_swap(a0, b0, false, false);
  auto r1 = __builtin_amdgcn_permlane16_swap(a1, b1, false, false);
  u32x4 v = {r0[0], r1[0], r0[1], r1[1]};
  return v;
}
DI void publish(char* ws, unsigned off, u64 tag, int tid) {
  asm volatile("s_waitcnt vmcnt(0)" ::: "memory");
  bar();
  if (tid == 0) __hip_atomic_store((GLB u64*)(ws + off), tag, __ATOMIC_RELAXED, __HIP_MEMORY_SCOPE_AGENT);
}
DI void poll(char* ws, unsigned off, int n, u64 tag, int lane) {
  GLB u64* p = (GLB u64*)(ws + off) + (lane < n ? lane : 0);
#pragma unroll 1
  for (int spin = 0; spin < SPIN_MAX; ++spin) {
    u64 v = __hip_atomic_load(p, __ATOMIC_RELAXED, __HIP_MEMORY_SCOPE_AGENT);
    if (__all(v == tag)) break;
    __builtin_amdgcn_s_sleep(1);
  }
  asm volatile("" ::: "memory");
}

DI float fma1(float a, float b, float c) { float d = __builtin_fmaf(a, b, c); asm("" : "+v"(d)); return d; }
DI float mul1(float a, float b) { float d = a * b; asm("" : "+v"(d)); return d; }
#define MF "v_mfma_f32_16x16x32_bf16 "
DI void mstep(f32x4 (&c)[4], const bf16x8 (&wt)[4], u32x4 a, int acc) {
  if (acc) asm volatile(MF "%0, %4, %8, %0\n\t" MF "%1, %5, %8, %1\n\t" MF "%2, %6, %8, %2\n\t" MF "%3, %7, %8, %3"
                        : "+v"(c[0]), "+v"(c[1]), "+v"(c[2]), "+v"(c[3]) : "v"(wt[0]), "v"(wt[1]), "v"(wt[2]), "v"(wt[3]), "v"(a));
  else asm volatile(MF "%0, %4, %8, 0\n\t" MF "%1, %5, %8, 0\n\t" MF "%2, %6, %8, 0\n\t" MF "%3, %7, %8, 0"
                    : "=&v"(c[0]), "=&v"(c[1]), "=&v"(c[2]), "=&v"(c[3]) : "v"(wt[0]), "v"(wt[1]), "v"(wt[2]), "v"(wt[3]), "v"(a));
}
DI f32x16 mfmaL(bf16x8 a, bf16x8 b, f32x16 c) { return __builtin_amdgcn_mfma_f32_32x32x16_bf16(a, b, c, 0, 0, 0); }
DI f32x16 mfmaS(u32x2 a, u32x2 b, f32x16 c) {
  return __builtin_amdgcn_mfma_f32_32x32x8bf16_1k(__builtin_bit_cast(s16x4, a), __builtin_bit_cast(s16x4, b), c, 0, 0, 0);
}
template <int SC> DI void dmai(unsigned lb, unsigned lo, unsigned voff, rsrc_t r, unsigned sb, unsigned so) {
  unsigned t;
  if (SC) asm volatile("s_add_u32 m0, %1, %2\n\ts_add_u32 %0, %5, %6\n\tbuffer_load_dwordx4 %3, %4, %0 offen sc1 lds"
                       : "=&s"(t) : "s"(lb), "n"(lo), "v"(voff), "s"(r), "s"(sb), "n"(so) : "memory", "scc");
  else asm volatile("s_add_u32 m0, %1, %2\n\ts_add_u32 %0, %5, %6\n\tbuffer_load_dwordx4 %3, %4, %0 offen lds"
                    : "=&s"(t) : "s"(lb), "n"(lo), "v"(voff), "s"(r), "s"(sb), "n"(so) : "memory", "scc");
}
template <int V> struct IC { static constexpr int value = V; };
template <int A, int B, class F> DI void sfor(F&& f) { if constexpr (A < B) { f(IC<A>{}); sfor<A + 1, B>(f); } }

constexpr unsigned harea(int t) { return t == 3 ? 32768u : 65536u + 32768u * t; }
constexpr unsigned warea(int j) { return j >= 3 && j <= 5 ? harea(j - 3) : 0u; }
constexpr int NW = 6;
constexpr int oH(int t, int i) { return 16 + t * 4 + i; }
constexpr int oW(int j, int i) { return 32 + j * 4 + i; }
constexpr int oA(int t, int j) { return 64 + t * 8 + j; }
constexpr int oX(int i) { return 8 + i; }
constexpr int oS = 1, oF = 2, oP = 3;
constexpr int vm_at(int pos) {
  if (pos < 0) return pos == -1 ? oP : oW(0, pos + 5);
  const int t = pos / 33, n = pos % 33;
  if (t > 0 && n == 21) return oS;
  if (t == 0) {
    if (n % 3 == 1 && n <= 19) return oH(1, 1) + n / 3;
    if (n >= 23 && n % 3 == 2) return oW(1, (n - 23) / 3);
  } else if (t == 1) {
    if (n % 3 == 1 && n <= 10) return oH(3, n / 3);
    if (n % 3 == 1 && n <= 22) return oW(3, (n - 13) / 3);
    if (n >= 24 && n % 2 == 0 && n < 32) return oW(2, (n - 24) / 2);
  } else if (t == 2) {
    if (n % 3 == 1 && n <= 10) return oW(4, n / 3);
    if (n == 13) return oF;
    if (n >= 24 && n % 2 == 0 && n < 32) return oX((n - 24) / 2);
  } else if (t == 3) {
    if (n >= 1 && n <= 7 && (n & 1)) return oW(5, n / 2);
    if (n >= 10 && n <= 16 && !(n & 1)) return oW(6, (n - 10) / 2);
    if (n >= 18 && n != 21 && !(n & 1) && n <= 30) return oA(0, (n - 18) / 2);
  }
  return 0;
}
constexpr int younger(int op, int pos) {
  int c = -1;
  for (int p = -5; p < pos; ++p) { if (c >= 0 && vm_at(p)) ++c; if (vm_at(p) == op) c = 0; }
  return c;
}
constexpr int rd_at(int pos) {
  const int t = pos / 33, n = pos % 33;
  if (t == 0) return n == 14 ? 0 : n == 15 ? 1 : n == 17 ? 2 : n == 18 ? 3 : -1;
  if (t == 1) return n == 11 ? 4 : n == 12 ? 5 : n == 14 ? 6 : n == 15 ? 7 : -1;
  if (t == 3) return n >= 4 && n < 16 ? 4 + n : -1;
  return -1;
}

#define SPA 1
#define SPD 2
constexpr int pf_at(int s) { return s >= SPA && (s - SPA) % SPD == 0 && (s - SPA) / SPD < 4 ? (s - SPA) / SPD : -1; }
constexpr int first1(int s) { return s == 0 ? 15 : s == 1 ? 22 : s == 2 ? 29 : 5 * s + 20; }
constexpr int after1(int id, int at) {
  int c = id < 10 ? 9 - id + 1 + 5 : id < 15 ? 14 - id : -1;
  for (int s = 0; s < at; ++s) {
    if (s == 4) { if (c >= 0) ++c; if (id == 100) c = 0; }
    for (int p = first1(s); p < first1(s + 1); ++p)
      if (p < 40 || p % 5 != 4) { if (c >= 0) ++c; if (p == id) c = 0; }
    if (c >= 0) c += pf_at(s) >= 0;
  }
  return c;
}

__global__ __launch_bounds__(512, 2) void fused_block(const u16* __restrict__ x, const u16* __restrict__ wqkv,
                                                      const u16* __restrict__ wo, const u16* __restrict__ wg,
                                                      const u16* __restrict__ wu, const u16* __restrict__ wd,
                                                      u16* __restrict__ out, char* __restrict__ ws) {
  const int tid = threadIdx.x, lane = tid & 63;
  const unsigned w = rfl(tid >> 6);
  const unsigned l15 = lane & 15, g = lane >> 4, q = (lane >> 2) & 3, p4 = lane & 3;
  const unsigned b = blockIdx.x, xcd = b & 7, bi = b >> 3;
  const rsrc_t rWS = mk(ws, WS_END);
  TS(0);
#define PAD(i)
  const unsigned rt = xcd * 4 + (bi & 3), cg = bi >> 2;
  const unsigned panel = (xcd >> 1) * 4 + (bi & 3), fg = (xcd & 1) * 7 + (bi >> 2);
  const unsigned off3 = bi < 28 ? 0u : 0x7f000000u;
  const unsigned prow = lane >> 3, pp = lane & 7;
  const unsigned fN = ((prow >> 1) & 1) | ((w & 1) << 1);
  const unsigned swzN = (((pp >> 1) ^ fN) << 5) | ((pp & 1) << 4);
  const unsigned frN = ((q >> 1) & 1) | ((g & 1) << 1);
  const unsigned trowN = (8 * g + q) * 128 + p4 * 8;
  const u32x4 ones = {0x3f803f80u, 0x3f803f80u, 0x3f803f80u, 0x3f803f80u};
  const rsrc_t rX = mk(x, S_ * D_ * 2);
  const unsigned vW3 = (8 * w + prow) * (F_ * 2) + fg * 256 + swzN + off3;
  const rsrc_t rG = mk(wg, D_ * F_ * 2), rU = mk(wu, D_ * F_ * 2);
  auto dma_q = [&](int qi, int i) {
    dma(((qi + 1) & 1) * 65536 + (i >> 1) * 16384 + (w + 8 * (i & 1)) * 1024, (i & 4) ? vW3 ^ 64 : vW3, (i & 4) ? rU : rG,
        (128 * qi + 64 * (i & 1)) * (F_ * 2) + ((i >> 1) & 1) * 128);
  };

  u64 tag, epoch;
  unsigned f1;
  {
    const unsigned rg = (xcd >> 1) * 8 + (bi & 7), hg = (xcd & 1) * 4 + (bi >> 3);
    const unsigned rot = ((bi & 7) + 2 * (bi >> 3)) & 7;
    const rsrc_t rW = mk(wqkv, D_ * 1536 * 2), rO = mk(wo, D_ * D_ * 2);
    u32x4 xrow;
    ld16(xrow, lane * 16, rX, (rg * 64 + hg * 8 + w) * 1024);
    const int r0 = 8 * w + prow;
    const unsigned vX0 = max((int)rg * 64 - 16 + r0, 0) * 1024 + ((pp ^ ((r0 >> 1) & 7)) << 4);
    const unsigned vX1 = (rg * 64 + 48 + 2 * w + (prow & 1)) * 1024 + ((pp ^ w) << 4);
    const unsigned vW = (8 * w + prow) * 3072 + hg * 128 + swzN;
    const unsigned vO = (8 * w + prow) * 1024 + cg * 128 + swzN;
    const u64 mX = w == 0 ? ~0xffull : ~0ull;
    auto piece = [&](int G, int i) {
      if (G < 8) {
        const unsigned st = (G + rot) & 7, sb = (G & 3) * SLOT;
        if (i == 0) dma_m(sb + w * 1024, vX0, rX, st * 128, mX);
        else if (i == 1) dma_m(sb + 8192 + w * 256, vX1, rX, st * 128, 0xffffull);
        else dma(sb + 10240 + (i - 2) * 8192 + w * 1024, vW, rW, st * 64 * 3072 + (i - 2) * 1024);
      } else if (i == 4) {
      } else if (G < 10) {
        dma((G - 8) * 32768 + (w + 8 * i) * 1024, vO, rO, (256 * (G - 8) + 64 * i) * 1024);
      } else {
        dma_q(0, 4 * (G - 10) + i);
      }
    };
#pragma unroll
    for (int i = 0; i < 5; ++i) piece(0, i);
    asm volatile("s_barrier" ::: "memory");
#pragma unroll
    for (int i = 0; i < 5; ++i) piece(1, i);
    TS(20);
    if (tid < 32) *(LDSP u32x4*)(smem + (tid >> 3) * SLOT + (tid & 7) * 16) = ones;
    epoch = *(const CST u64*)(ws + WS_EPOCH);
    tag = epoch * 0x9E3779B97F4A7C15ull + 0x632BE59BD9B4E019ull;
    const unsigned tag32 = (unsigned)(tag >> 32);
    {
      vm_tie<10>(xrow);
      TS(21);
      float s1 = 0.f, s2 = 0.f;
#pragma unroll
      for (int j = 0; j < 4; ++j) { s1 = dot2(xrow[j], 0x3f803f80u, s1); s2 = dot2(xrow[j], xrow[j], s2); }
      float s = sum2rows(s1, s2);
      s += dppf<0x128>(s); s += dppf<0x124>(s); s += dppf<0x122>(s); s += dppf<0x121>(s);
      const float m = __int_as_float(__builtin_amdgcn_readlane(__float_as_int(s), 0)) * (1.f / 512.f);
      const float e2 = __int_as_float(__builtin_amdgcn_readlane(__float_as_int(s), 16)) * (1.f / 512.f);
      const float rs = __builtin_amdgcn_rsqf(fmaxf(e2 - m * m, 0.f) + 1e-5f);
      const u32x4 gr = {__float_as_uint(m), tag32, __float_as_uint(rs), tag32};
      st16<16>(gr, rWS, lane == 0 ? 0u : 0x7ff00000u, WS_S1 + (rg * 64 + hg * 8 + w) * 16);
    }
    const unsigned hw = w & 3, half = w >> 2;
    const unsigned xa = (l15 * 128 + ((g ^ (l15 >> 1)) << 4) + half * 4096) ^ (half * 64);
    const unsigned wa = (10240 + trowN + ((hw ^ frN) << 5)) + half * 4096;
    const unsigned xb = xa ^ 64, wb = wa + 4096 - half * 8192;
    const unsigned par = l15 & 1;
    f32x4 mc, mp, mp1;
#pragma unroll
    for (int r = 0; r < 4; ++r) {
      const bool pm = (r & 1) == par, cur = 4 * g + r <= l15;
      mc[r] = (pm && cur) ? 0.f : -4e9f;
      mp[r] = (pm && !cur) ? 0.f : -4e9f;
      mp1[r] = (rg == 0 && half == 0) ? -4e9f : mp[r];
    }
    const unsigned se0 = par ? 0x01000c0cu : 0x0c0c0100u, se1 = par ? 0x03020c0cu : 0x0c0c0302u;
    const bool c0 = (4 * g + par) <= l15, c1 = (4 * g + 2 + par) <= l15;
    u32x4 sel = {c0 ? 0x0c0c0c0cu : se0, c1 ? 0x0c0c0c0cu : se1, c0 ? se0 : 0x0c0c0c0cu, c1 ? se1 : 0x0c0c0c0cu};
    unsigned vS = WS_S1 + max((int)(rg * 64 - 16 + half * 32 + (g < 3 ? g : 0) * 16 + l15), 0) * 16;
    tie(mc); tie(mp); tie(mp1); tie(sel); tie(vS);

    f32x4 aQ[2], aK[3], aV[3], aC = {0, 0, 0, 0};
#pragma unroll
    for (int i = 0; i < 3; ++i) { aK[i] = {0, 0, 0, 0}; aV[i] = {0, 0, 0, 0}; }
    aQ[0] = {0, 0, 0, 0}; aQ[1] = {0, 0, 0, 0};
    bf16x8 xf[2][2][3];
    s16x4 wl[2][2][3], wh[2][2][3];
    auto rd = [&](int s, int r) {
      const int kk = r / 9, i = r % 9;
      const unsigned sb = (s & 3) * SLOT;
      if (i < 3) xf[s & 1][kk][i] = lds_b128(sb + (kk ? xb : xa) + i * 2048);
      else {
        const int m = ((i - 3) / 2 + 1) % 3;
        const unsigned a = sb + (kk ? wb : wa) + m * 8192;
        if ((i - 3) & 1) wh[s & 1][kk][m] = lds_tr(a + 512); else wl[s & 1][kk][m] = lds_tr(a);
      }
    };
    auto mm = [&](int s, int n) {
      const int c = s & 1, kk = n > 8, i = kk ? n - 9 : n;
      const bf16x8 wq = cat(wl[c][kk][0], wh[c][kk][0]), wk = cat(wl[c][kk][1], wh[c][kk][1]), wv = cat(wl[c][kk][2], wh[c][kk][2]);
      SB();
      if (i < 3) aK[i] = mfma32(wk, xf[c][kk][i], aK[i]);
      else if (i < 6) aV[i - 3] = mfma32(xf[c][kk][i - 3], wv, aV[i - 3]);
      else if (i < 8) aQ[i - 6] = mfma32(wq, xf[c][kk][i - 5], aQ[i - 6]);
      else aC = mfma32(wq, asbf(ones), aC);
      SB();
    };
    TS(22);
    wait_vm<6>();
    TS(23);
    bar();
    TS(1);
    PAD(1);
#pragma unroll
    for (int r = 0; r < 18; ++r) rd(0, r);
    SB();
#pragma unroll
    for (int i = 0; i < 5; ++i) piece(2, i);
    u32x4 sl;
    const unsigned nP = hw == 3 ? 8 : 7, vP = lane < nP ? ((b * 2 + half) * nP + lane) * 128 : 0x7f000000u;
    SB();
    auto part = [&](int s, int n0, int n1) __attribute__((always_inline)) {
#pragma unroll
      for (int n = n0; n < n1; ++n) {
        mm(s, n);
        const int slot = n % 3 == 2 ? n / 3 + (n > 13) : n == 13 ? 4 : n == 16 ? 6 : -1;
        const int first = first1(s), cnt = first1(s + 1) - first;
        if (slot >= 0 && slot < cnt) piece((first + slot) / 5, (first + slot) % 5);
        if (n == 16 && pf_at(s) >= 0) touch(vP + pf_at(s) * (D_ * F_ / 2), mk(hw == 0 ? wg : hw == 1 ? wu : hw == 2 ? wd : wo, hw == 3 ? D_ * D_ * 2 : D_ * F_ * 2));
        if (n % 3 != 2 && n < 13 && s < 7) { rd(s + 1, 2 * (n - n / 3)); rd(s + 1, 2 * (n - n / 3) + 1); }
      }
    };
#pragma unroll
    for (int s = 0; s < 6; ++s) {
      if (s == 0) wait_vm<after1(9, 0)>(); else if (s == 1) wait_vm<after1(14, 1)>(); else if (s == 2) wait_vm<after1(19, 2)>();
      else if (s == 3) wait_vm<after1(24, 3)>(); else if (s == 4) wait_vm<after1(29, 4)>(); else wait_vm<after1(34, 5)>();
      bar();
      if (s == 4) { TS(2); ld16c<0>(sl, vS, rWS, 0); }
      part(s, 0, 17);
    }
    vm_tie<after1(100, 6)>(sl);
    bar();
    part(6, 0, 14);
#pragma unroll 1
    for (int spin = 0; spin < SPIN_MAX; ++spin) {
      if (__all(sl[1] == tag32 && sl[3] == tag32)) break;
      __builtin_amdgcn_s_sleep(1);
      ld16c<0>(sl, vS, rWS, 0);
      vm_tie<0>(sl);
    }
    *(LDSP unsigned*)(smem + L_FS + w * 512 + lane * 4) = sl[0];
    *(LDSP unsigned*)(smem + L_FS + w * 512 + 256 + lane * 4) = sl[2];
    part(6, 14, 17);
    bar();
    part(7, 0, 17);
    TS(3);
    *(LDSP f32x4*)(smem + L_CX + w * 2304 + lane * 16) = aC;
    *(LDSP f32x4*)(smem + L_CX + w * 2304 + 1024 + lane * 16) = aK[0];
    *(LDSP float*)(smem + L_CX + w * 2304 + 2048 + lane * 4) = aV[0][0];
    float sm[3], sr[3];
    f32x4 vm[3], vr[3];
#pragma unroll
    for (int t = 0; t < 3; ++t) {
      sm[t] = *(LDSP float*)(smem + L_FS + w * 512 + t * 64 + l15 * 4);
      sr[t] = *(LDSP float*)(smem + L_FS + w * 512 + 256 + t * 64 + l15 * 4);
      vm[t] = *(LDSP f32x4*)(smem + L_FS + w * 512 + t * 64 + g * 16);
      vr[t] = *(LDSP f32x4*)(smem + L_FS + w * 512 + 256 + t * 64 + g * 16);
    }
    wait_vm<after1(48, 8)>();
    bar();
    const f32x4 csq = aC + *(LDSP f32x4*)(smem + L_CX + (w ^ 4) * 2304 + lane * 16);
    const f32x4 csk = *(LDSP f32x4*)(smem + L_CX + hw * 2304 + 1024 + g * 256);
    const float csv = *(LDSP float*)(smem + L_CX + hw * 2304 + 2048 + l15 * 4);
    u32x2 qb[2], kb[3], vb[3];
#pragma unroll
    for (int t = 0; t < 3; ++t) {
      const f32x4 kv = (aK[t] - sm[t] * csk) * sr[t];
      kb[t] = {pk(kv[0], kv[1]), pk(kv[2], kv[3])};
      if (t > 0) { const f32x4 qv = (aQ[t - 1] - sm[t] * csq) * sr[t]; qb[t - 1] = {pk(qv[0], qv[1]), pk(qv[2], qv[3])}; }
      const f32x4 vv = (aV[t] - vm[t] * csv) * vr[t];
      vb[t] = {pk(vv[0], vv[1]), pk(vv[2], vv[3])};
    }
    float l0[2], l1[2];
#pragma unroll
    for (int t = 0; t < 2; ++t) {
      const f32x4 sc = mfma16(kb[t + 1], qb[t], mc), sp = mfma16(kb[t], qb[t], t ? mp : mp1);
      l0[t] = fmaxf(fmaxf(fmaxf(sc[0], sc[1]), sp[0]), sp[1]);
      l1[t] = fmaxf(fmaxf(fmaxf(sc[2], sc[3]), sp[2]), sp[3]);
    }
    f32x2 mx;
    {
      const f32x2 a = swp16(fmaxf(l0[0], l1[0]), fmaxf(l0[1], l1[1]));
      const float m1 = fmaxf(a[0], a[1]);
      const f32x2 c = swp32(m1, m1);
      const float m2 = fmaxf(c[0], c[1]) * (-0.25f * 1.44269504f);
      mx = swp16(m2, m2);
    }
    unsigned ob[2][2];
#pragma unroll
    for (int t = 0; t < 2; ++t) {
      const float e0 = __builtin_amdgcn_exp2f(fmaf(l0[t], 0.25f * 1.44269504f, mx[t])), e1 = __builtin_amdgcn_exp2f(fmaf(l1[t], 0.25f * 1.44269504f, mx[t]));
      const unsigned e = pk(e0, e1);
      u32x4 pf;
#pragma unroll
      for (int j = 0; j < 4; ++j) pf[j] = __builtin_amdgcn_perm(0u, e, sel[j]);
      const u32x4 vf = {vb[t][0], vb[t][1], vb[t + 1][0], vb[t + 1][1]};
      const f32x4 z = {0, 0, 0, 0};
      const f32x4 den = mfma32(asbf(ones), asbf(pf), z);
      const f32x4 o = mfma32(asbf(vf), asbf(pf), z) * __builtin_amdgcn_rcpf(den[0]);
      ob[t][0] = pk(o[0], o[1]); ob[t][1] = pk(o[2], o[3]);
    }
    {
      const u32x4 ov = swap_pair(ob[0][0], ob[0][1], ob[1][0], ob[1][1]);
      const unsigned head = hg * 4 + hw;
      const unsigned off = (g & 1) * 16384 + ((g >> 1) * 16 + l15) * 16;
      st16<16>(ov, rWS, off, WS_O + ((rg * 4 + half * 2) * 16 + (head >> 1)) * 1024 + (head & 1) * 512);
    }
    TS(4);
    f1 = WS_F1 + (rg * 16 + hg) * 8;
  }

  bf16x8 fw[32];
  const unsigned aq = ((g & 1) * 2 + (w >> 2)) * 16384 + (8 * (g >> 1) + q) * 128 + (((w & 3) ^ (q >> 1) ^ (g & 2) ^ ((g & 1) << 1)) << 5)
                      + (((p4 & 1) << 1) | (p4 >> 1)) * 8;
  auto rdq = [&](int qi) {
    unsigned a = aq + ((qi + 1) & 1) * 65536;
    tie(a);
#pragma unroll
    for (int ks = 0; ks < 8; ++ks) fw[8 * qi + ks] = lds_tr2(a + ks * 2048, a + ks * 2048 + 512);
    SB();
  };
  {
    const unsigned gp = ((g & 1) << 1) | (g >> 1);
    const unsigned m2 = (w >> 2) * 2 + (w & 1), kh = (w >> 1) & 1;
    u32x4 xr;
    ld16(xr, (m2 * 16 + l15) * 1024 + gp * 16, rX, rt * 65536 + cg * 128 + kh * 64);
    bf16x8 wf[8][4];
#pragma unroll
    for (int ks = 0; ks < 8; ++ks)
#pragma unroll
      for (int c = 0; c < 4; ++c) {
        const unsigned a = kh * 32768 + trowN + ((c ^ (2 * kh) ^ frN) << 5) + ks * 4096;
        wf[ks][c] = lds_tr2(a, a + 512);
      }
    SB();
    vm_tie<0>(xr);
    publish(ws, f1, tag, tid);
    TS(5);
    PAD(5);
    rdq(0);
#pragma unroll
    for (int i = 0; i < 8; ++i) dma_q(1, i);
    bar();
    TS(6);
    poll(ws, WS_F1 + (rt * 16 + kh * 4) * 8, 4, tag, lane);
    TS(7);
    u32x4 of[8];
    const unsigned ob = WS_O + ((rt * 4 + m2) * 16 + kh * 8) * 1024;
    ld16c<0>(of[0], lane * 16, rWS, ob); ld16c<1024>(of[1], lane * 16, rWS, ob); ld16c<2048>(of[2], lane * 16, rWS, ob); ld16c<3072>(of[3], lane * 16, rWS, ob);
    ld16c<0>(of[4], lane * 16, rWS, ob + 4096); ld16c<1024>(of[5], lane * 16, rWS, ob + 4096); ld16c<2048>(of[6], lane * 16, rWS, ob + 4096); ld16c<3072>(of[7], lane * 16, rWS, ob + 4096);
    f32x4 acc[4];
#pragma unroll
    for (int c = 0; c < 4; ++c) acc[c] = {0, 0, 0, 0};
#pragma unroll
    for (int ks = 0; ks < 8; ++ks) {
      vm_tie<7>(of[ks]);
#pragma unroll
      for (int c = 3; c >= 0; --c) acc[c] = mfma32(wf[ks][c], asbf(of[ks]), acc[c]);
      SB();
      dma_q(2, ks);
    }
    TS(8);
    *(LDSP f32x4*)(smem + L_RED + w * 2048 + lane * 16) = acc[2];
    *(LDSP f32x4*)(smem + L_RED + w * 2048 + 1024 + lane * 16) = acc[3];
    bar();
    const f32x4 k0 = acc[0] + *(LDSP f32x4*)(smem + L_RED + (w ^ 2) * 2048 + lane * 16);
    const f32x4 k1 = acc[1] + *(LDSP f32x4*)(smem + L_RED + (w ^ 2) * 2048 + 1024 + lane * 16);
    const u32x4 av = swap_pair(pk(k0[0], k0[1]), pk(k0[2], k0[3]), pk(k1[0], k1[1]), pk(k1[2], k1[3]));
    u32x4 hreg;
#pragma unroll
    for (int j = 0; j < 4; ++j) hreg[j] = pk(blo(av[j]) + blo(xr[j]), bhi(av[j]) + bhi(xr[j]));
    st16<16>(hreg, rWS, (gp * 16 + l15) * 16, WS_H + ((rt * 4 + m2) * 16 + cg * 2 + kh) * 1024);
    float s1 = 0.f, s2 = 0.f;
#pragma unroll
    for (int j = 0; j < 4; ++j) { s1 = dot2(hreg[j], 0x3f803f80u, s1); s2 = dot2(hreg[j], hreg[j], s2); }
    const float ss = sum2rows(s1, s2);
    __builtin_amdgcn_raw_buffer_store_b32(__float_as_uint(ss), rWS, g < 2 ? l15 * 8 + g * 4 : 0x7ff00000u, WS_S2 + ((rt * 4 + m2) * 16 + cg * 2 + kh) * 128, 16);
    SB();
    rdq(1);
    publish(ws, WS_F2 + (rt * 16 + cg) * 8, tag, tid);
    TS(9);
  }

  const rsrc_t rD = mk(wd, D_ * F_ * 2);
  const unsigned vD0 = prow * 1024 + cg * 128 + ((((pp >> 1) ^ ((prow >> 1) & 1)) << 5) | ((pp & 1) << 4)), vD1 = vD0 ^ 64;
  const unsigned lw = w * 1024, sD = w * (7 * 32 * 1024);
  auto dma_wd = [&](int j, int i, unsigned area) { dmai<0>(lw, area + i * 8192, (i & 1) ? vD1 : vD0, rD, sD, (j * 32 + i * 8) * 1024); };
  bf16x8 wdf[7][4];
  unsigned tw;
  auto mktw = [&](unsigned base) {
    unsigned ln = lane; tie(ln);
    tw = base + lw + (ln >> 4) * 8192 + ((ln >> 2) & 3) * 128 + (ln & 3) * 8 + ((((ln >> 3) & 1) | ((ln >> 3) & 2)) << 5);
    tie(tw);
  };
  auto rdw = [&](int j, int c, unsigned area) {
    unsigned a = tw ^ (c << 5);
    tie(a);
    wdf[j][c] = lds_tr2(a + (area & 65535), a + (area & 65535) + 512);
  };
  auto tiew = [&](int j) { asm volatile("" : "+v"(wdf[j][0]), "+v"(wdf[j][1]), "+v"(wdf[j][2]), "+v"(wdf[j][3])); };
  u32x2 fl;
  auto ldf = [&]() { unsigned ln = lane; tie(ln); ld8c<0>(fl, min(ln, 13u) * 8, rWS, WS_F3 + (rt >> 1) * 128); };
  const unsigned ab4 = WS_A + (rt * 4 * 56 + w * 7) * 1024;
  u32x4 af[3][7];
  auto lda = [&](int t, int j) {
    const unsigned so = ab4 + t * (56 * 1024) + (j >> 2) * 4096;
    u32x4& a = af[t % 3][j];
    if ((j & 3) == 0) ld16c<0>(a, lane * 16, rWS, so); else if ((j & 3) == 1) ld16c<1024>(a, lane * 16, rWS, so);
    else if ((j & 3) == 2) ld16c<2048>(a, lane * 16, rWS, so); else ld16c<3072>(a, lane * 16, rWS, so);
  };
  auto open = [&](const u32x4& a) { return __any(max(max(max(a[0], a[1]), a[2]), a[3]) == ~0u); };
  unsigned hv[4];
  auto ldx = [&](int t) {
    unsigned ln = lane; tie(ln);
    const unsigned fc = ln >> 4, fq = (ln >> 2) & 3, fr = (ln >> 1) & 1, fp = ln & 1;
    ld4c(hv[t], (fc >> 1) * 1024 + ((fc & 1) * 2 + (fq >> 1)) * 256 + fr * 16 + (fq & 1) * 8 + fp * 4, rWS, WS_H + ((rt * 4 + t) * 16 + cg * 2) * 1024 + w * 32);
  };
  auto phase4 = [&]() __attribute__((always_inline)) {
    f32x4 acc[2][4];
    auto chk = [&](int t, int j) {
      u32x4& a = af[t % 3][j];
      if (__builtin_expect(open(a), 0)) {
#pragma unroll 1
        for (int spin = 0; spin < SPIN_MAX; ++spin) {
          __builtin_amdgcn_s_sleep(2);
          lda(t, j);
          vm_tie<0>(a);
          if (!open(a)) break;
        }
      }
      SB();
    };
    auto mm = [&](int t, int j) { chk(t, j); mstep(acc[t & 1], wdf[j], af[t % 3][j], j); SB(); };
    constexpr int PA[4] = {3, 4, 5, 0};
    const unsigned pw0 = lw + (g * 16 + (l15 ^ (2 * g))) * 16, pw1 = pw0 ^ 128;
    auto put = [&](int t) {
      auto& a = acc[t & 1];
      asm volatile("s_nop 7" : "+v"(a[0]), "+v"(a[1]), "+v"(a[2]), "+v"(a[3]));
#pragma unroll
      for (int c = 0; c < 4; ++c) *(LDSP f32x4*)(smem + warea(PA[t]) + c * 8192 + ((c & 1) ? pw1 : pw0)) = acc[t & 1][c];
      SB();
    };
    const unsigned fc = lane >> 4, fq = (lane >> 2) & 3, fr = (lane >> 1) & 1, fp = lane & 1;
    const unsigned pr = fc * 8192 + (fq * 16 + ((2 * w + fr) ^ (2 * fq + 8 * (fc & 1)))) * 16 + fp * 8;
    const rsrc_t rOut = mk(out, S_ * D_ * 2);
    const unsigned vOut = fr * 1024 + fc * 32 + fq * 8 + fp * 4;
    auto fin = [&](int t) {
      f32x2 v[8];
#pragma unroll
      for (int s = 0; s < 8; ++s) v[s] = *(LDSP f32x2*)(smem + warea(PA[t]) + pr + s * 1024);
      const f32x2 r = ((v[0] + v[1]) + (v[2] + v[3])) + ((v[4] + v[5]) + (v[6] + v[7]));
      const unsigned f = pk(r[0], r[1]);
      __builtin_amdgcn_raw_buffer_store_b32(pk(blo(f) + blo(hv[t]), bhi(f) + bhi(hv[t])), rOut, vOut, (rt * 64 + t * 16 + 2 * w) * 1024 + cg * 128, 0);
    };
    TS(16);
#pragma unroll
    for (int j = 0; j < 7; ++j) { vm_tie<13>(af[0][j]); mm(0, j); lda(2, j); }
    TS(61);
    sfor<0, 7>([&](auto J) { constexpr int j = J.value; vm_tie<13 - j>(af[1][j]); mm(1, j); if (j == 0) put(0); });
    TS(62);
#pragma unroll
    for (int j = 0; j < 7; ++j) { vm_tie<6>(af[2][j]); mm(2, j); lda(3, j); if (j == 0) put(1); }
    put(2);
    TS(63);
    bar();
    fin(0); fin(1); fin(2);
    SB();
    sfor<0, 7>([&](auto J) { constexpr int j = J.value; vm_tie<9 - j>(af[0][j]); mm(3, j); });
    TS(17);
    put(3);
    bar();
    fin(3);
    TS(18);
    PAD(18);
  };
  {
    const unsigned r2 = (lane >> 4) & 1, hk = lane >> 5;
    unsigned ao = r2 * (4 * 56 * 1024) + hk * 256 + l15 * 16 + off3;
    const unsigned ab = WS_A + (panel * 8 * 56 + fg * 4 + (w >> 1)) * 1024 + (w & 1) * 512;
#pragma unroll
    for (int i = 0; i < 8; ++i) dma_q(3, i);
    {
      u32x4 none = {~0u, ~0u, ~0u, ~0u};
      tie(none);
#pragma unroll
      for (int t = 0; t < 4; ++t) st16<16>(none, rWS, ao, ab + t * (56 * 1024));
    }
    rdq(2);
    const unsigned s1 = ((l15 >> 2) & 1) == (g & 1) ? 0x3f803f80u : 0u;
    const u32x4 sel = {s1, s1, s1, s1};
    f32x4 cs = {0, 0, 0, 0};
#pragma unroll
    for (int ks = 0; ks < 24; ++ks) cs = mfma32(asbf(sel), fw[ks], cs);
    SB();
    wait_vm<4>();
    bar();
    TS(10);
    rdq(3);
#pragma unroll
    for (int ks = 24; ks < 32; ++ks) cs = mfma32(asbf(sel), fw[ks], cs);
    SB();
    bar();
    u32x2 ax;
    {
      const unsigned c1 = pk(cs[0], cs[0]);
      const float d = cs[0] - blo(c1);
      ax = {hk ? pk(d, d) : c1, 0u};
      tie(ax);
    }
    TS(14);
    const bool last = b == 255;
    poll(ws, (last ? WS_F1 + w * 512 : WS_F2 + panel * 256) + ((lane >> 3) & (last ? 3 : 1)) * 128, 8, tag, lane & 7);
    TS(11);
    u32x2 sp[4];
    {
      const unsigned so = WS_S2 + (panel * 8 + w) * 2048, vs = g * 512 + l15 * 8;
      ld8c<0>(sp[0], vs, rWS, so); ld8c<128>(sp[1], vs, rWS, so); ld8c<256>(sp[2], vs, rWS, so); ld8c<384>(sp[3], vs, rWS, so);
    }
    unsigned vH = r2 * 65536 + hk * 256 + l15 * 16 + off3;
    tie(vH); tie(ao);
    const unsigned hb = WS_H + panel * 8 * 16384 + w * 512;
    auto dma_h = [&](int t, int i) { dmai<1>(lw, harea(t) + i * 8192, vH, rWS, hb, t * 16384 + i * 4096); };
#pragma unroll
    for (int i = 0; i < 5; ++i) dma_h(i >> 2, i & 3);
#pragma unroll
    for (int i = 0; i < 4; ++i) dma_wd(0, i, 0);
    asm volatile("s_waitcnt vmcnt(9)" : "+v"(sp[0]), "+v"(sp[1]), "+v"(sp[2]), "+v"(sp[3]) :: "memory");
    {
      const f32x2 s = (__builtin_bit_cast(f32x2, sp[0]) + __builtin_bit_cast(f32x2, sp[1])) + (__builtin_bit_cast(f32x2, sp[2]) + __builtin_bit_cast(f32x2, sp[3]));
      const float r = sum2rows(s[0], s[1]) * (1.f / 512.f);
      const f32x2 me = swp16(r, r);
      const float var = fmaxf(me[1] - me[0] * me[0], 0.f) + 1e-5f;
      const float m1 = blo(pk(-me[0], 0.f));
      const u32x2 st = {pk(m1, -me[0] - m1), __float_as_uint(__builtin_amdgcn_rsqf(var) * -1.44269504f)};
      *(LDSP u32x2*)(smem + L_ST + ((w & 3) * 32 + (w >> 2) * 16 + l15) * 8) = st;
    }
    wait_vm<4>();
    bar();
    TS(12);
    PAD(12);
    unsigned ha[3] = {lane * 16u, lane * 16u + 65536, lane * 16u + 131072};
    bf16x8 hf[NW];
    auto rdh = [&](int f) { const unsigned a = harea(f >> 5) + (f & 31) * 1024; hf[f % NW] = lds_b128(ha[a >> 16] + (a & 65535)); };
    u32x2 st[4];
    tie(ha[1]);
#pragma unroll
    for (int t = 0; t < 4; ++t) st[t] = *(LDSP u32x2*)(smem + L_ST + t * 256 + (lane & 31) * 8);
#pragma unroll
    for (int f = 0; f < NW; ++f) rdh(f);
    SB();
    {
      const u64 fv = last ? epoch + 1 : tag;
      const u32x2 f2 = {(unsigned)fv, (unsigned)(fv >> 32)};
      __builtin_amdgcn_raw_buffer_store_b64(f2, rWS, tid ? 0x7ff00000u : last ? 0u : off3, last ? WS_EPOCH : WS_F3 + (panel * 16 + fg) * 8, 16);
    }
    SB();
    f32x16 acc[2];
    float kk;
    u32x4 av;
    auto epi = [&](int t, int k) {
      f32x16& a = acc[t & 1];
      const float sc = __uint_as_float(st[t][1]);
      if (k == 0) kk = __builtin_amdgcn_rcpf(sc);
      else if (k == 1) kk = mul1(kk, 1.44269504f * 1.44269504f);
      else {
        const int i = (k - 2) / 7, o = (k - 2) % 7;
        if (o == 0) { if (i < 8) a[i] = mul1(a[i], sc); }
        else if (o == 1) { if (i >= 1 && i < 9) a[i - 1] = fma1(a[i - 1], kk, kk); }
        else if (o == 2) { if (i < 8) a[8 + i] = mul1(a[i], a[8 + i]); }
        else if (o == 3) { if (i < 8) { float e = __builtin_amdgcn_exp2f(a[i]); tie(e); a[i] = e; } }
        else if (o == 4) { if (i >= 2) a[i - 2] = mul1(a[i - 2], a[8 + i - 2]); }
        else if (o == 5) { if (i >= 1 && i < 9) { float e = __builtin_amdgcn_rcpf(a[i - 1]); tie(e); a[i - 1] = e; } }
        else if (i >= 3 && (i & 1)) { unsigned v = min(pk(a[i - 3], a[i - 2]), ~1u); tie(v); av[(i - 3) >> 1] = v; }
      }
    };
    constexpr int NE = 72;
    sfor<0, 4 * 33>([&](auto P) __attribute__((always_inline)) {
      constexpr int pos = P.value, t = pos / 33, n = pos % 33, op = vm_at(pos), rd = rd_at(pos);
      if constexpr (n == 0) {
        const f32x16 z = {0, 0, 0, 0, 0, 0, 0, 0, 0, 0, 0, 0, 0, 0, 0, 0};
        const u32x2 bx = {st[t][0], st[t][0]};
        acc[t & 1] = mfmaS(ax, bx, z);
        tie(acc[t & 1]);
        SB();
        if constexpr (t > 0) {
          TS3(24 + 2 * t);
          wait_vm<younger(t == 3 ? oH(3, 3) : oH(t + 1, 0), pos)>();
          asm volatile("s_barrier" ::: "memory");
          TS3(25 + 2 * t);
        }
        if constexpr (t == 1) tie(ha[2]);
        if constexpr (t == 2) tie(ha[0]);
      } else {
        constexpr int f = t * 32 + n - 1;
        acc[t & 1] = mfmaL(fw[n - 1], hf[f % NW], acc[t & 1]);
        tie(acc[t & 1]);
        SB();
        if constexpr (f + NW < 128) rdh(f + NW);
      }
      SB();
      if constexpr (t > 0 && n >= 2 && n < 20) {
#pragma unroll
        for (int k = 4 * (n - 2); k < 4 * (n - 1); ++k) epi(t - 1, k);
      }
      SB();
      if constexpr (rd >= 0) {
        if constexpr ((rd & 3) == 0) { wait_vm<younger(oW(rd >> 2, 3), pos)>(); if (rd != 4 && rd != 16) mktw(warea(rd >> 2) & ~65535u); }
        rdw(rd >> 2, rd & 3, warea(rd >> 2));
      }
      if constexpr (op == oS) st16<16>(av, rWS, ao, ab + (t - 1) * (56 * 1024));
      else if constexpr (op == oF) ldf();
      else if constexpr (op >= oA(0, 0)) lda((op - 64) >> 3, op & 7);
      else if constexpr (op >= oW(0, 0)) {
        constexpr int j = (op - 32) >> 2;
        if constexpr ((op & 3) == 0 && (j == 1 || j == 2 || j == 6)) tiew(j == 6 ? 2 : j - 1);
        dma_wd(j, op & 3, warea(j));
      }
      else if constexpr (op >= oH(0, 0)) dma_h((op - 16) >> 2, op & 3);
      else if constexpr (op >= oX(0)) ldx(op - 8);
      if constexpr (t == 2 && n == 23) {
        vm_tie<younger(oF, pos + 1)>(fl);
        if (__builtin_expect(!__all(fl[0] == (unsigned)tag && fl[1] == (unsigned)(tag >> 32)), 0)) {
#pragma unroll 1
          for (int spin = 0; spin < SPIN_MAX; ++spin) {
            __builtin_amdgcn_s_sleep(1);
            ldf();
            vm_tie<0>(fl);
            if (__all(fl[0] == (unsigned)tag && fl[1] == (unsigned)(tag >> 32))) break;
          }
        }
      }
      SB();
    });
    TS(13);
    PAD(13);
    mktw(131072);
    asm volatile("s_waitcnt vmcnt(%4)" : "+v"(hv[0]), "+v"(hv[1]), "+v"(hv[2]), "+v"(hv[3]) : "n"(younger(oW(5, 3), 132)) : "memory");
#pragma unroll
    for (int c = 0; c < 4; ++c) rdw(5, c, warea(5));
    SB();
#pragma unroll
    for (int k = 0; k < NE; ++k) epi(3, k);
    st16<16>(av, rWS, ao, ab + 3 * (56 * 1024));
    SB();
    TS(15);
    mktw(0);
    wait_vm<younger(oW(6, 3), 132) + 1>();
#pragma unroll
    for (int j = 0; j < 7; ++j) { if (j < 4) rdw(6, j, 0); SB(); lda(1, j); }
    phase4();
  }
}

__global__ void tiny_a(char* ws) { if (threadIdx.x == 0) __hip_atomic_store((GLB u64*)(ws + 1024), (u64)__builtin_amdgcn_s_memrealtime(), __ATOMIC_RELAXED, __HIP_MEMORY_SCOPE_AGENT); }
__global__ void tiny_b(char* ws) { u64 t0 = __hip_atomic_load((GLB u64*)(ws + 1024), __ATOMIC_RELAXED, __HIP_MEMORY_SCOPE_AGENT);
  for (int i = 0; i < 100000 && __builtin_amdgcn_s_memrealtime() - t0 < 8000; ++i) __builtin_amdgcn_s_sleep(1); }
extern "C" void kernel_launch(void* const* d_in, const int* in_sizes, int n_in, void* d_out, int out_size, void* d_ws,
                              size_t ws_size, hipStream_t stream) {
  (void)hipFuncSetAttribute((const void*)fused_block, hipFuncAttributeMaxDynamicSharedMemorySize, LDS_BYTES);
  tiny_a<<<1, 64, 0, stream>>>((char*)d_ws);
  fused_block<<<256, 512, LDS_BYTES, stream>>>((const u16*)d_in[0], (const u16*)d_in[1], (const u16*)d_in[2], (const u16*)d_in[4],
                                               (const u16*)d_in[5], (const u16*)d_in[6], (u16*)d_out, (char*)d_ws);
  tiny_b<<<1, 64, 0, stream>>>((char*)d_ws);
}
```

```cpp
#include <hip/hip_runtime.h>
#include <hip/hip_bf16.h>

typedef unsigned short u16;
typedef unsigned long long u64;
typedef __attribute__((ext_vector_type(8))) __bf16 bf16x8;
typedef __attribute__((ext_vector_type(2))) __bf16 bf16x2;
typedef __attribute__((ext_vector_type(4))) float f32x4;
typedef __attribute__((ext_vector_type(2))) float f32x2;
typedef __attribute__((ext_vector_type(16))) float f32x16;
typedef __attribute__((ext_vector_type(4))) short s16x4;
typedef __attribute__((ext_vector_type(8))) short s16x8;
typedef __attribute__((ext_vector_type(4))) unsigned u32x4;
typedef __attribute__((ext_vector_type(2))) unsigned u32x2;
#define LDSP __attribute__((address_space(3)))
#define GLB __attribute__((address_space(1)))
#define CST __attribute__((address_space(4)))
#define DI __device__ __forceinline__
#define SB() __builtin_amdgcn_sched_barrier(0)
typedef __amdgpu_buffer_rsrc_t rsrc_t;

constexpr int S_ = 2048, D_ = 512, F_ = 1792;
constexpr unsigned WS_EPOCH = 0;
constexpr unsigned WS_F1 = 4096;
constexpr unsigned WS_F2 = WS_F1 + 4096;
constexpr unsigned WS_S1 = 16384;
constexpr unsigned WS_F3 = 49152;
constexpr unsigned WS_O = 131072;
constexpr unsigned WS_H = WS_O + 2097152;
constexpr unsigned WS_A = WS_H + 2097152;
constexpr unsigned WS_S2 = WS_A + 128 * 56 * 1024;
constexpr unsigned WS_END = WS_S2 + 128 * 4096;
constexpr unsigned SLOT = 36864;
constexpr unsigned L_FS = 159744;
constexpr unsigned L_CX = 139264;
constexpr unsigned L_RED = 143360;
constexpr int SPIN_MAX = 1 << 20;
constexpr unsigned L_ST = 57344;
constexpr unsigned LDS_BYTES = 163840;

extern __shared__ __attribute__((aligned(16))) char smem[];
#define PRIO(x) do { SB(); __builtin_amdgcn_s_setprio(x); SB(); } while (0)

DI unsigned rfl(unsigned v) { return __builtin_amdgcn_readfirstlane(v); }
DI rsrc_t mk(const void* p, unsigned bytes) { return __builtin_amdgcn_make_buffer_rsrc((void*)p, (short)0, (int)bytes, 0x00020000); }
DI void dma(unsigned lds, unsigned voff, rsrc_t r, unsigned soff) {
  asm volatile("s_mov_b32 m0, %0\n\ts_nop 0\n\tbuffer_load_dwordx4 %1, %2, %3 offen lds"
               :: "s"(lds + (unsigned)(size_t)(LDSP char*)smem), "v"(voff), "s"(r), "s"(soff) : "memory");
}
DI void dma_m(unsigned lds, unsigned voff, rsrc_t r, unsigned soff, u64 mask) {
  asm volatile("s_mov_b32 m0, %0\n\ts_mov_b64 exec, %4\n\tbuffer_load_dwordx4 %1, %2, %3 offen lds\n\ts_mov_b64 exec, -1"
               :: "s"(lds + (unsigned)(size_t)(LDSP char*)smem), "v"(voff), "s"(r), "s"(soff), "s"(mask) : "memory");
}
template <int OFF> DI void ld16c(u32x4& d, unsigned voff, rsrc_t r, unsigned soff) {
  asm volatile("buffer_load_dwordx4 %0, %1, %2, %3 offen offset:%4 sc1" : "=v"(d) : "v"(voff), "s"(r), "s"(soff), "n"(OFF) : "memory");
}
template <int OFF> DI void ld8c(u32x2& d, unsigned voff, rsrc_t r, unsigned soff) {
  asm volatile("buffer_load_dwordx2 %0, %1, %2, %3 offen offset:%4 sc1" : "=v"(d) : "v"(voff), "s"(r), "s"(soff), "n"(OFF) : "memory");
}
DI void ld4c(unsigned& d, unsigned voff, rsrc_t r, unsigned soff) {
  asm volatile("buffer_load_dword %0, %1, %2, %3 offen sc1" : "=v"(d) : "v"(voff), "s"(r), "s"(soff) : "memory");
}
DI void ld16(u32x4& d, unsigned voff, rsrc_t r, unsigned soff) {
  asm volatile("buffer_load_dwordx4 %0, %1, %2, %3 offen" : "=v"(d) : "v"(voff), "s"(r), "s"(soff) : "memory");
}
DI void touch(unsigned voff, rsrc_t r) {
  asm volatile("buffer_load_dword v255, %0, %1, 0 offen" :: "v"(voff), "s"(r) : "memory", "v255");
}
DI void touch_l(unsigned lds, unsigned voff, rsrc_t r, unsigned soff) {
  asm volatile("s_mov_b32 m0, %0\n\ts_nop 0\n\tbuffer_load_dword %1, %2, %3 offen lds" :: "s"(lds), "v"(voff), "s"(r), "s"(soff) : "memory");
}
template <int AUX> DI void st16(u32x4 v, rsrc_t r, unsigned voff, unsigned soff) {
  __builtin_amdgcn_raw_buffer_store_b128(v, r, voff, soff, AUX);
  asm volatile("s_nop 1" :: "v"(v) : "memory");
}
template <int N> DI void wait_vm() { asm volatile("s_waitcnt vmcnt(%0)" :: "n"(N) : "memory"); }
template <int N, class T> DI void vm_tie(T& a) { asm volatile("s_waitcnt vmcnt(%1)" : "+v"(a) : "n"(N) : "memory"); }
template <int N, class T> DI void vm_tie(T& a, T& b) { asm volatile("s_waitcnt vmcnt(%2)" : "+v"(a), "+v"(b) : "n"(N) : "memory"); }
template <class T> DI void tie(T& a) { asm volatile("" : "+v"(a)); }
DI void wait_lds() { asm volatile("" ::: "memory"); __builtin_amdgcn_s_waitcnt(0xC07F); asm volatile("" ::: "memory"); }
DI void bar() { wait_lds(); asm volatile("s_barrier" ::: "memory"); }
template <class T> DI LDSP T* lds(unsigned a) { return (LDSP T*)a; }
DI bf16x8 lds_b128(unsigned a) { return *lds<bf16x8>(a); }
DI s16x4 lds_tr(unsigned a) { return __builtin_amdgcn_ds_read_tr16_b64_v4i16(lds<s16x4>(a)); }
DI bf16x8 cat(s16x4 lo, s16x4 hi) { return __builtin_bit_cast(bf16x8, __builtin_shufflevector(lo, hi, 0, 1, 2, 3, 4, 5, 6, 7)); }
DI bf16x8 lds_tr2(unsigned a0, unsigned a1) { return cat(lds_tr(a0), lds_tr(a1)); }
DI bf16x8 asbf(u32x4 v) { return __builtin_bit_cast(bf16x8, v); }
DI f32x4 mfma32(bf16x8 a, bf16x8 b, f32x4 c) { return __builtin_amdgcn_mfma_f32_16x16x32_bf16(a, b, c, 0, 0, 0); }
DI f32x4 mfma16(u32x2 a, u32x2 b, f32x4 c) {
  return __builtin_amdgcn_mfma_f32_16x16x16bf16_1k(__builtin_bit_cast(s16x4, a), __builtin_bit_cast(s16x4, b), c, 0, 0, 0);
}
DI unsigned pk(float a, float b) { f32x2 f = {a, b}; return __builtin_bit_cast(unsigned, __builtin_convertvector(f, bf16x2)); }
DI float blo(unsigned v) { return __uint_as_float(v << 16); }
DI float bhi(unsigned v) { return __uint_as_float(v & 0xffff0000u); }
DI float dot2(unsigned a, unsigned b, float c) {
  return __builtin_amdgcn_fdot2_f32_bf16(__builtin_bit_cast(bf16x2, a), __builtin_bit_cast(bf16x2, b), c, false);
}
DI f32x2 swp16(float a, float b) {
  auto r = __builtin_amdgcn_permlane16_swap(__float_as_uint(a), __float_as_uint(b), false, false);
  f32x2 v = {__uint_as_float(r[0]), __uint_as_float(r[1])}; return v;
}
DI f32x2 swp32(float a, float b) {
  auto r = __builtin_amdgcn_permlane32_swap(__float_as_uint(a), __float_as_uint(b), false, false);
  f32x2 v = {__uint_as_float(r[0]), __uint_as_float(r[1])}; return v;
}
DI float sum2rows(float a, float b) { f32x2 p = swp16(a, b); float s = p[0] + p[1]; f32x2 q = swp32(s, s); return q[0] + q[1]; }
template <int CTRL> DI float dppf(float v) { return __int_as_float(__builtin_amdgcn_update_dpp(0, __float_as_int(v), CTRL, 0xf, 0xf, true)); }
DI u32x4 swap_pair(unsigned a0, unsigned a1, unsigned b0, unsigned b1) {
  auto r0 = __builtin_amdgcn_permlane16_swap(a0, b0, false, false);
  auto r1 = __builtin_amdgcn_permlane16_swap(a1, b1, false, false);
  u32x4 v = {r0[0], r1[0], r0[1], r1[1]};
  return v;
}
DI void publish(char* ws, unsigned off, u64 tag, int tid) {
  asm volatile("s_waitcnt vmcnt(0)" ::: "memory");
  bar();
  if (tid < 2) __hip_atomic_store((GLB u64*)(ws + off), tag, __ATOMIC_RELAXED, __HIP_MEMORY_SCOPE_AGENT);
}
DI unsigned max4(u32x4 a, unsigned m) { return max(max(max(a[0], a[1]), m), max(a[2], a[3])); }
DI void poll(char* ws, unsigned off, int n, u64 tag, int lane) {
  GLB u64* p = (GLB u64*)(ws + off) + (lane < n ? lane : 0);
#pragma unroll 1
  for (int spin = 0; spin < SPIN_MAX; ++spin) {
    u64 v = __hip_atomic_load(p, __ATOMIC_RELAXED, __HIP_MEMORY_SCOPE_AGENT);
    if (__all(v == tag)) break;
    __builtin_amdgcn_s_sleep(1);
  }
  asm volatile("" ::: "memory");
}

DI float fma1(float a, float b, float c) { float d = __builtin_fmaf(a, b, c); asm("" : "+v"(d)); return d; }
DI float add1(float a, float b) { float d; asm("v_add_f32 %0, %1, %2" : "=v"(d) : "v"(a), "v"(b)); return d; }
DI float mul1(float a, float b) { float d = a * b; asm("" : "+v"(d)); return d; }
#define MF "v_mfma_f32_16x16x32_bf16 "
DI void mstep(f32x4 (&c)[4], const bf16x8 (&wt)[4], u32x4 a, int acc) {
  if (acc) asm volatile(MF "%0, %4, %8, %0\n\t" MF "%1, %5, %8, %1\n\t" MF "%2, %6, %8, %2\n\t" MF "%3, %7, %8, %3"
                        : "+v"(c[0]), "+v"(c[1]), "+v"(c[2]), "+v"(c[3]) : "v"(wt[0]), "v"(wt[1]), "v"(wt[2]), "v"(wt[3]), "v"(a));
  else asm volatile(MF "%0, %4, %8, 0\n\t" MF "%1, %5, %8, 0\n\t" MF "%2, %6, %8, 0\n\t" MF "%3, %7, %8, 0"
                    : "=&v"(c[0]), "=&v"(c[1]), "=&v"(c[2]), "=&v"(c[3]) : "v"(wt[0]), "v"(wt[1]), "v"(wt[2]), "v"(wt[3]), "v"(a));
}
DI f32x16 mfmaL(bf16x8 a, bf16x8 b, f32x16 c) { return __builtin_amdgcn_mfma_f32_32x32x16_bf16(a, b, c, 0, 0, 0); }
DI f32x16 mfmaS(u32x2 a, u32x2 b, f32x16 c) {
  return __builtin_amdgcn_mfma_f32_32x32x8bf16_1k(__builtin_bit_cast(s16x4, a), __builtin_bit_cast(s16x4, b), c, 0, 0, 0);
}
template <int SC> DI void dmai(unsigned lb, unsigned lo, unsigned voff, rsrc_t r, unsigned sb, unsigned so) {
  unsigned t;
  if (SC) asm volatile("s_add_u32 m0, %1, %2\n\ts_add_u32 %0, %5, %6\n\tbuffer_load_dwordx4 %3, %4, %0 offen sc1 lds"
                       : "=&s"(t) : "s"(lb), "n"(lo), "v"(voff), "s"(r), "s"(sb), "n"(so) : "memory", "scc");
  else asm volatile("s_add_u32 m0, %1, %2\n\ts_add_u32 %0, %5, %6\n\tbuffer_load_dwordx4 %3, %4, %0 offen lds"
                    : "=&s"(t) : "s"(lb), "n"(lo), "v"(voff), "s"(r), "s"(sb), "n"(so) : "memory", "scc");
}
template <int V> struct IC { static constexpr int value = V; };
template <int A, int B, class F> DI void sfor(F&& f) { if constexpr (A < B) { f(IC<A>{}); sfor<A + 1, B>(f); } }

constexpr unsigned harea(int t) { return t == 3 ? 32768u : 65536u + 32768u * t; }
constexpr unsigned warea(int j) { return j >= 3 && j <= 5 ? harea(j - 3) : 0u; }
constexpr int NW = 6;
constexpr int oH(int t, int i) { return 16 + t * 4 + i; }
constexpr int oW(int j, int i) { return 32 + j * 4 + i; }
constexpr int oA(int t, int j) { return 64 + t * 8 + j; }
constexpr int oX(int i) { return 8 + i; }
constexpr int oC(int t) { return 12 + t; }
constexpr int oZ(int i) { return 60 + i; }
constexpr int oS = 1, oF = 2, oP = 3, oT = 4, oG = 5, oQ = 6, oO = 7, oE = 12;
constexpr int putp(int t, int c) { return t < 3 ? 28 * t + 30 + 3 * c : 103 + 4 * c; }
constexpr int barp(int t) { return putp(t, 3) + 3; }
constexpr int lda_at(int p) {
  const int t = p / 28, m = p % 28;
  return t < 3 && m % 4 == 1 ? oA(t + 1, m / 4) : 0;
}
constexpr int free_at(int p) { return lda_at(p) ? p + 1 : p; }
constexpr int SM = free_at(14);
constexpr int CHK = 116;
constexpr int vm_at(int pos) {
  if (pos >= 132) {
    const int p = pos - 132;
    if (p == SM) return oS;
    if (p == free_at(barp(0) + 18) || p == free_at(barp(1) + 18)) return oO;
    return lda_at(p);
  }
  const int t = pos / 33, n = pos % 33;
  if (t == 0) {
    constexpr int t0[33] = {0, oH(1, 1), oZ(0), oH(1, 2), oZ(1), oH(1, 3), oZ(2), oH(2, 0), oZ(3), oE, oW(0, 0), 0, oC(2), oW(0, 1), 0, 0, oW(0, 2),
                            0, 0, oW(0, 3), oT, 0, 0, oH(2, 1), 0, 0, oH(2, 2), 0, 0, oC(3), 0, 0, 0};
    return t0[n];
  }
  if (pos == 72) return oP;
  if (pos == 101) return oQ;
  if (pos == 108) return oF;
  if (pos == 118) return oG;
  if (n == (t == 1 ? 24 : 21)) return oS;
  if (t == 1) {
    if (n == 1 || n == 3) return oH(2, 3) + n / 2;
    if (n >= 6 && n <= 12 && !(n & 1)) return oW(1, (n - 6) / 2);
    if (n >= 14 && n <= 20 && !(n & 1)) return oW(3, (n - 14) / 2);
    if (n == 22 || n == 26) return oH(3, 1) + (n - 22) / 4;
  } else if (t == 2) {
    if (n == 2) return oH(3, 3);
    if (n % 3 == 1 && n <= 10) return oW(4, n / 3);
    if (n >= 13 && n <= 19 && (n & 1)) return oW(2, (n - 13) / 2);
    if (n >= 24 && n % 2 == 0 && n < 32) return oX((n - 24) / 2);
  } else if (t == 3) {
    if (n >= 1 && n <= 7 && (n & 1)) return oW(5, n / 2);
    if (n >= 10 && n <= 16 && !(n & 1)) return oW(6, (n - 10) / 2);
    if (n >= 18 && !(n & 1) && n <= 30) return oA(0, (n - 18) / 2);
  }
  return 0;
}
constexpr int younger(int op, int pos) {
  int c = -1;
  for (int p = 0; p < pos; ++p) { if (c >= 0 && vm_at(p)) ++c; if (vm_at(p) == op) c = 0; }
  return c;
}
constexpr int count(int op) { int c = 0; for (int p = 0; p < 132 + 112; ++p) c += vm_at(p) == op; return c; }
constexpr bool complete() {
  for (int op = 2; op < 96; ++op) {
    const bool want = op != oO && op != 13 && !(op >= oH(0, 0) && op <= oH(1, 0)) && !(op >= 64 && (op & 7) == 7);
    if (want && count(op) != 1) return false;
  }
  return count(oS) == 4 && count(oO) == 2;
}
static_assert(complete());
constexpr int before(int pos) { int c = 0; for (int p = 0; p < pos; ++p) c += vm_at(p) != 0; return c; }
constexpr int rd_at(int pos) {
  const int t = pos / 33, n = pos % 33;
  if (t == 1 || t == 2) return n >= 1 && n <= 4 ? 4 * (t - 1) + n - 1 : -1;
  if (t == 3) return n >= 4 && n < 16 ? 4 + n : -1;
  return -1;
}

constexpr int first1(int s) { constexpr int f[9] = {14, 19, 24, 29, 34, 38, 42, 46, 50}; return f[s]; }
constexpr int last1(int G) { return G < 2 ? 5 * G + 4 : 4 * G + 5; }
constexpr int piece_at(int s, int n) {
  const int slot = n % 3 == 2 ? n / 3 + (n > 13) : n == 13 ? 4 : n == 16 ? 6 : -1, p = first1(s) + slot;
  return slot >= 0 && p < first1(s + 1) ? p : -1;
}
constexpr int slow_pos(int i) { constexpr int f[6] = {  -11, 119,   85, -11, 33,   34}; return f[i]; }
constexpr int after1(int id, int at, int stop = -1) {
  int c = -1, r = -2;
  auto see = [&](int op) { if (op == stop) r = c; if (c >= 0) ++c; if (op == id) c = 0; };
  auto slow = [&](int pos) { for (int i = 0; i < 6; ++i) if (slow_pos(i) == pos) see(100 + i); };
  for (int p = 0; p <= 14; ++p) {
    slow(-1 - p);
    if (p == 10 && at < 0) return c;
    if (p < 14) see(p);
  }
  for (int s = 0; s < at; ++s)
    for (int n = 0; n < 17; ++n) {
      if (piece_at(s, n) >= 0) see(piece_at(s, n));
      slow(17 * s + n);
    }
  return stop < 0 ? c : r;
}
constexpr int cmin(int a, int b) { return a < b ? a : b; }
constexpr int wait1(int s) {
  int c = s < 7 ? after1(last1(s + 1), s) : 63;
  if (s == 7) c = cmin(c, after1(102, s));
  return c;
}

__global__ __launch_bounds__(512, 2) void fused_block(const u16* __restrict__ x, const u16* __restrict__ wqkv,
                                                      const u16* __restrict__ wo, const u16* __restrict__ wg,
                                                      const u16* __restrict__ wu, const u16* __restrict__ wd,
                                                      u16* __restrict__ out, char* __restrict__ ws) {
  const int tid = threadIdx.x, lane = tid & 63;
  const unsigned w = rfl(tid >> 6);
  const unsigned l15 = lane & 15, g = lane >> 4, q = (lane >> 2) & 3, p4 = lane & 3;
  const unsigned b = blockIdx.x, xcd = b & 7, bi = b >> 3;
  const rsrc_t rWS = mk(ws, WS_END);
  const unsigned rt = xcd * 4 + (bi & 3), cg = (bi >> 2) ^ 4;
  const unsigned panel = (xcd >> 1) * 4 + (bi & 3), fg = (xcd & 1) * 7 + (bi >> 2);
  const unsigned off3 = bi < 28 ? 0u : 0x7f000000u;
  const unsigned prow = lane >> 3, pp = lane & 7;
  const unsigned fN = ((prow >> 1) & 1) | ((w & 1) << 1);
  const unsigned swzN = (((pp >> 1) ^ fN) << 5) | ((pp & 1) << 4);
  const unsigned frN = ((q >> 1) & 1) | ((g & 1) << 1);
  const unsigned trowN = (8 * g + q) * 128 + p4 * 8;
  const u32x4 ones = {0x3f803f80u, 0x3f803f80u, 0x3f803f80u, 0x3f803f80u};
  const rsrc_t rX = mk(x, S_ * D_ * 2);
  const unsigned vW3 = (8 * w + prow) * (F_ * 2) + fg * 256 + swzN + off3;
  const rsrc_t rG = mk(wg, D_ * F_ * 2), rU = mk(wu, D_ * F_ * 2);
  auto dma_q = [&](int qi, int i) {
    dma(((qi + 1) & 1) * 65536 + (i >> 1) * 16384 + (w + 8 * (i & 1)) * 1024, (i & 4) ? vW3 ^ 64 : vW3, (i & 4) ? rU : rG,
        (128 * qi + 64 * (i & 1)) * (F_ * 2) + ((i >> 1) & 1) * 128);
  };

  const unsigned ab = WS_A + (panel * 8 * 56 + fg * 4 + (w >> 1)) * 1024 + (w & 1) * 512;
  auto mkao = [&]() {
    unsigned ln = lane; tie(ln);
    unsigned a = ((ln >> 4) & 1) * (4 * 56 * 1024) + (ln >> 5) * 256 + (ln & 15) * 16 + off3;
    tie(a);
    return a;
  };
  u64 tag, epoch;
  unsigned f1;
  {
    const unsigned rg = (xcd >> 1) * 8 + (bi & 7), hg = (xcd & 1) * 4 + (bi >> 3);
    const unsigned rot = (((bi & 7) + 2 * (bi >> 3)) & 7);
    const rsrc_t rW = mk(wqkv, D_ * 1536 * 2), rO = mk(wo, D_ * D_ * 2);
    epoch = *(const CST u64*)(ws + WS_EPOCH);
    const int r0 = 8 * w + prow;
    const unsigned vX0 = max((int)rg * 64 - 16 + r0, 0) * 1024 + ((pp ^ ((r0 >> 1) & 7)) << 4);
    const unsigned vX1 = (rg * 64 + 48 + (r0 & 15)) * 1024 + ((pp ^ ((r0 >> 1) & 7)) << 4);
    const unsigned vW = (8 * w + prow) * 3072 + hg * 128 + swzN;
    const unsigned vO = (8 * w + prow) * 1024 + cg * 128 + swzN;
    const u64 mX = w == 0 ? ~0xffull : ~0ull;
    auto piece = [&](int p) __attribute__((always_inline)) {
      const int G = p < 10 ? p / 5 : (p - 2) / 4, i = p < 10 ? p % 5 : (p - 2) % 4;
      if (G < 8) {
        const unsigned st = (G + rot) & 7, sb = (G & 3) * SLOT;
        if (i == 0) dma_m(sb + w * 1024, vX0, rX, st * 128, mX);
        else if (i == 4) dma((w >> 1) * SLOT + 8192 + G * 2048 + (w & 1) * 1024, vX1, rX, ((4 * G + (w >> 1) + rot) & 7) * 128);
        else dma(sb + 12288 + (i - 1) * 8192 + w * 1024, vW, rW, st * 64 * 3072 + (i - 1) * 1024);
      } else if (G < 10) {
        dma((G - 8) * 32768 + (w + 8 * i) * 1024, vO, rO, (256 * (G - 8) + 64 * i) * 1024);
      } else {
        dma_q(0, 4 * (G - 10) + i);
      }
    };
    const unsigned hw = w & 3, half = w >> 2;
    rsrc_t rP;
    auto pull = [&](int k) __attribute__((always_inline)) {
      unsigned ln = lane; tie(ln);
      const unsigned qd = k ? 1 + (ln >= 7) + (ln >= 14) : 0;
      const unsigned v3 = (b >> 1) * (F_ * 2) + ((b & 1) * 14 + half * 7 + ln) * 128 + qd * (D_ * F_ / 2 - 7 * 128) + (k ? 7 * 128 : 0);
      unsigned v = hw >= 2 ? ((b * 2 + half + (hw == 2 ? (b >> 5) * 160 : 0)) * 8 + ln) * 128 : v3;
      touch(ln < (k ? (hw >= 2 ? 0u : 21u) : hw >= 2 ? 8u : 7u) ? v : 0x7f000000u, rP);
    };
    u32x4 xrow, sl;
    unsigned vS, tag32;
    auto pois = [&]() __attribute__((always_inline)) {
      u32x4 no = {~0u, ~0u, ~0u, ~0u};
      unsigned ln = lane;
      tie(no); tie(ln);
      st16<16>(no, rWS, ln * 16 + (w >= (cg < 2 ? 6 : 4) ? 0x7f000000u : 0u), WS_H + ((rt * 4 + (w >> 1)) * 16 + cg * 2 + (w & 1)) * 1024);
      SB();
    };
    auto slow = [&](int pos) __attribute__((always_inline)) {
      if (pos == slow_pos(0)) pull(0);
      if (pos == slow_pos(1)) pull(1);
      if (pos == slow_pos(2)) ld16c<0>(sl, vS, rWS, 0);
      if (pos == slow_pos(3)) ld16(xrow, lane * 16, rX, (rg * 64 + hg * 8 + w) * 1024);
      if (pos == slow_pos(4)) {
        static_assert(after1(103, 8, 104) >= 0);
        vm_tie<after1(103, 8, 104)>(xrow);
        float s1 = 0.f, s2 = 0.f;
#pragma unroll
        for (int j = 0; j < 4; ++j) { s1 = dot2(xrow[j], 0x3f803f80u, s1); s2 = dot2(xrow[j], xrow[j], s2); }
        float s = sum2rows(s1, s2);
        s += dppf<0x128>(s); s += dppf<0x124>(s); s += dppf<0x122>(s); s += dppf<0x121>(s);
        const float m = __int_as_float(__builtin_amdgcn_readlane(__float_as_int(s), 0)) * (1.f / 512.f);
        const float e2 = __int_as_float(__builtin_amdgcn_readlane(__float_as_int(s), 16)) * (1.f / 512.f);
        const float rs = __builtin_amdgcn_rsqf(fmaxf(e2 - m * m, 0.f) + 1e-5f);
        const u32x4 gr = {__float_as_uint(m), tag32, __float_as_uint(rs), tag32};
        st16<16>(gr, rWS, lane == 0 ? 0u : 0x7ff00000u, WS_S1 + (rg * 64 + hg * 8 + w) * 16);
        SB();
      }
      if (pos == slow_pos(5)) pois();
    };
    auto lead = [&](int p0, int p1) __attribute__((always_inline)) {
#pragma unroll
      for (int p = p0; p < p1; ++p) { piece(p); slow(-2 - p); }
    };
    slow(-1);
    lead(0, 5);
    asm volatile("s_barrier" ::: "memory");
    SB();
    if (tid < 32) *(LDSP u32x4*)(smem + (tid >> 3) * SLOT + (tid & 7) * 16) = ones;
    rP = mk((hw & 2) ? ((hw & 1) ? wo : wd) : ((hw & 1) ? wu : wg), hw == 3 ? D_ * D_ * 2 : D_ * F_ * 2);
    unsigned xa = (l15 * 128 + ((g ^ (l15 >> 1)) << 4) + half * 4096) ^ (half * 64);
    unsigned wa = (12288 + trowN + ((hw ^ frN) << 5)) + half * 4096;
    unsigned xb = xa ^ 64, wb = wa + 4096 - half * 8192;
    tie(xa); tie(xb); tie(wa); tie(wb);
    const unsigned par = l15 & 1;
    f32x4 mc, mp, mp1;
#pragma unroll
    for (int r = 0; r < 4; ++r) {
      const bool pm = (r & 1) == par, cur = 4 * g + r <= l15;
      mc[r] = (pm && cur) ? 0.f : -4e9f;
      mp[r] = (pm && !cur) ? 0.f : -4e9f;
      mp1[r] = (rg == 0 && half == 0) ? -4e9f : mp[r];
    }
    const unsigned se0 = par ? 0x01000c0cu : 0x0c0c0100u, se1 = par ? 0x03020c0cu : 0x0c0c0302u;
    const bool c0 = (4 * g + par) <= l15, c1 = (4 * g + 2 + par) <= l15;
    u32x4 sel = {c0 ? 0x0c0c0c0cu : se0, c1 ? 0x0c0c0c0cu : se1, c0 ? se0 : 0x0c0c0c0cu, c1 ? se1 : 0x0c0c0c0cu};
    vS = WS_S1 + max((int)(rg * 64 - 16 + half * 32 + (g < 3 ? g : 0) * 16 + l15), 0) * 16;
    tie(mc); tie(mp); tie(mp1); tie(sel); tie(vS);
    SB();
    lead(5, 10);
    SB();
    tag = epoch * 0x9E3779B97F4A7C15ull + 0x632BE59BD9B4E019ull;
    tag32 = (unsigned)(tag >> 32);

    f32x4 aQ[2], aK[3], aV[3], aC = {0, 0, 0, 0};
#pragma unroll
    for (int i = 0; i < 3; ++i) { aK[i] = {0, 0, 0, 0}; aV[i] = {0, 0, 0, 0}; }
    aQ[0] = {0, 0, 0, 0}; aQ[1] = {0, 0, 0, 0};
    bf16x8 xf[2][2][3];
    s16x4 wl[2][2][3], wh[2][2][3];
    auto rd = [&](int s, int r) {
      const int kk = r / 9, i = r % 9;
      const unsigned sb = (s & 3) * SLOT;
      if (i < 3) xf[s & 1][kk][i] = lds_b128(sb + (kk ? xb : xa) + i * 2048 + (i == 2 && s >= 4 ? half * 2048 : 0));
      else {
        const int m = ((i - 3) / 2 + 1) % 3;
        const unsigned a = sb + (kk ? wb : wa) + m * 8192;
        if ((i - 3) & 1) wh[s & 1][kk][m] = lds_tr(a + 512); else wl[s & 1][kk][m] = lds_tr(a);
      }
    };
    auto mm = [&](int s, int n) {
      const int c = s & 1, kk = n > 8, i = kk ? n - 9 : n;
      const bf16x8 wq = cat(wl[c][kk][0], wh[c][kk][0]), wk = cat(wl[c][kk][1], wh[c][kk][1]), wv = cat(wl[c][kk][2], wh[c][kk][2]);
      SB();
      if (i < 3) aK[i] = mfma32(wk, xf[c][kk][i], aK[i]);
      else if (i < 6) aV[i - 3] = mfma32(xf[c][kk][i - 3], wv, aV[i - 3]);
      else if (i < 8) aQ[i - 6] = mfma32(wq, xf[c][kk][i - 5], aQ[i - 6]);
      else aC = mfma32(wq, asbf(ones), aC);
      SB();
    };
    wait_vm<after1(4, -1)>();
    bar();
#pragma unroll
    for (int r = 0; r < 18; ++r) rd(0, r);
    SB();
    lead(10, 14);
    SB();
    auto part = [&](int s, int n0, int n1) __attribute__((always_inline)) {
#pragma unroll
      for (int n = n0; n < n1; ++n) {
        mm(s, n);
        if (piece_at(s, n) >= 0) piece(piece_at(s, n));
        slow(17 * s + n);
        if (n % 3 != 2 && n < 13 && s < 7) { rd(s + 1, 2 * (n - n / 3)); rd(s + 1, 2 * (n - n / 3) + 1); }
      }
    };
    sfor<0, 7>([&](auto S) __attribute__((always_inline)) {
      constexpr int s = S.value;
      static_assert(wait1(s) >= 0);
      wait_vm<wait1(s)>();
      bar();
      part(s, 0, 17);
    });
    static_assert(wait1(7) >= 0);
    vm_tie<wait1(7)>(sl);
    bar();
    part(7, 0, 14);
#pragma unroll 1
    for (int spin = 0; spin < SPIN_MAX; ++spin) {
      if (__all(sl[1] == tag32 && sl[3] == tag32)) break;
      __builtin_amdgcn_s_sleep(1);
      ld16c<0>(sl, vS, rWS, 0);
      vm_tie<0>(sl);
    }
    *(LDSP unsigned*)(smem + L_FS + w * 512 + lane * 4) = sl[0];
    *(LDSP unsigned*)(smem + L_FS + w * 512 + 256 + lane * 4) = sl[2];
    part(7, 14, 17);
    *(LDSP f32x4*)(smem + L_CX + w * 2304 + lane * 16) = aC;
    *(LDSP f32x4*)(smem + L_CX + w * 2304 + 1024 + lane * 16) = aK[0];
    *(LDSP float*)(smem + L_CX + w * 2304 + 2048 + lane * 4) = aV[0][0];
    float sm[3], sr[3];
    f32x4 vm[3], vr[3];
#pragma unroll
    for (int t = 0; t < 3; ++t) {
      sm[t] = *(LDSP float*)(smem + L_FS + w * 512 + t * 64 + l15 * 4);
      sr[t] = *(LDSP float*)(smem + L_FS + w * 512 + 256 + t * 64 + l15 * 4);
      vm[t] = *(LDSP f32x4*)(smem + L_FS + w * 512 + t * 64 + g * 16);
      vr[t] = *(LDSP f32x4*)(smem + L_FS + w * 512 + 256 + t * 64 + g * 16);
    }
    wait_vm<after1(last1(9), 8)>();
    bar();
    const f32x4 csq = aC + *(LDSP f32x4*)(smem + L_CX + (w ^ 4) * 2304 + lane * 16);
    const f32x4 csk = *(LDSP f32x4*)(smem + L_CX + hw * 2304 + 1024 + g * 256);
    const float csv = *(LDSP float*)(smem + L_CX + hw * 2304 + 2048 + l15 * 4);
    u32x2 qb[2], kb[3], vb[3];
    auto tail = [&](int k) __attribute__((always_inline)) { if (first1(8) + k < 50) { SB(); piece(first1(8) + k); SB(); } };
    tail(0);
#pragma unroll
    for (int t = 0; t < 3; ++t) {
      const f32x4 kv = (aK[t] - sm[t] * csk) * sr[t];
      kb[t] = {pk(kv[0], kv[1]), pk(kv[2], kv[3])};
      if (t > 0) { const f32x4 qv = (aQ[t - 1] - sm[t] * csq) * sr[t]; qb[t - 1] = {pk(qv[0], qv[1]), pk(qv[2], qv[3])}; }
      const f32x4 vv = (aV[t] - vm[t] * csv) * vr[t];
      vb[t] = {pk(vv[0], vv[1]), pk(vv[2], vv[3])};
      tail(1 + t);
    }
    static_assert(first1(8) >= 46);
    float l0[2], l1[2];
#pragma unroll
    for (int t = 0; t < 2; ++t) {
      const f32x4 sc = mfma16(kb[t + 1], qb[t], mc), sp = mfma16(kb[t], qb[t], t ? mp : mp1);
      l0[t] = fmaxf(fmaxf(fmaxf(sc[0], sc[1]), sp[0]), sp[1]);
      l1[t] = fmaxf(fmaxf(fmaxf(sc[2], sc[3]), sp[2]), sp[3]);
    }
    f32x2 mx;
    {
      const f32x2 a = swp16(fmaxf(l0[0], l1[0]), fmaxf(l0[1], l1[1]));
      const float m1 = fmaxf(a[0], a[1]);
      const f32x2 c = swp32(m1, m1);
      const float m2 = fmaxf(c[0], c[1]) * (-0.25f * 1.44269504f);
      mx = swp16(m2, m2);
    }
    unsigned ob[2][2];
#pragma unroll
    for (int t = 0; t < 2; ++t) {
      const float e0 = __builtin_amdgcn_exp2f(fmaf(l0[t], 0.25f * 1.44269504f, mx[t])), e1 = __builtin_amdgcn_exp2f(fmaf(l1[t], 0.25f * 1.44269504f, mx[t]));
      const unsigned e = pk(e0, e1);
      u32x4 pf;
#pragma unroll
      for (int j = 0; j < 4; ++j) pf[j] = __builtin_amdgcn_perm(0u, e, sel[j]);
      const u32x4 vf = {vb[t][0], vb[t][1], vb[t + 1][0], vb[t + 1][1]};
      const f32x4 z = {0, 0, 0, 0};
      const f32x4 den = mfma32(asbf(ones), asbf(pf), z);
      const f32x4 o = mfma32(asbf(vf), asbf(pf), z) * __builtin_amdgcn_rcpf(den[0]);
      ob[t][0] = pk(o[0], o[1]); ob[t][1] = pk(o[2], o[3]);
    }
    {
      const u32x4 ov = swap_pair(ob[0][0], ob[0][1], ob[1][0], ob[1][1]);
      const unsigned head = hg * 4 + hw;
      const unsigned off = (g & 1) * 16384 + ((g >> 1) * 16 + l15) * 16;
      st16<16>(ov, rWS, off, WS_O + ((rg * 4 + half * 2) * 16 + (head >> 1)) * 1024 + (head & 1) * 512);
    }
    f1 = WS_F1 + (rg * 16 + hg) * 8 + tid * (((rt - rg) * 16 + 8 + cg - hg) * 8 + WS_F2 - WS_F1);
  }

  bf16x8 fw[32];
  const unsigned aq = ((g & 1) * 2 + (w >> 2)) * 16384 + (8 * (g >> 1) + q) * 128 + (((w & 3) ^ (q >> 1) ^ (g & 2) ^ ((g & 1) << 1)) << 5)
                      + (((p4 & 1) << 1) | (p4 >> 1)) * 8;
  auto rdq = [&](int qi) {
    unsigned a = aq + ((qi + 1) & 1) * 65536;
    tie(a);
#pragma unroll
    for (int ks = 0; ks < 8; ++ks) fw[8 * qi + ks] = lds_tr2(a + ks * 2048, a + ks * 2048 + 512);
    SB();
  };
  const unsigned cs1 = ((l15 >> 2) & 1) == (g & 1) ? 0x3f803f80u : 0u;
  const u32x4 csel = {cs1, cs1, cs1, cs1};
  f32x4 cs = {0, 0, 0, 0};
  auto csum = [&](int k0, int k1) __attribute__((always_inline)) {
    SB();
#pragma unroll
    for (int ks = k0; ks < k1; ++ks) cs = mfma32(asbf(csel), fw[ks], cs);
    tie(cs);
    SB();
  };
  u32x2 fl;
  auto hasf = [&]() { return __all(fl[0] == (unsigned)tag && fl[1] == (unsigned)(tag >> 32)); };
  const bool last = b == 255;
  auto ld2 = [&](int k) {
    unsigned ln = lane; tie(ln);
    ld8c<0>(fl, ((ln >> 3) & (last && !k ? 3 : 1)) * 128 + (ln & 7) * 8, rWS, k ? WS_F2 + panel * 256 : WS_F2 + 64 + (last ? w * 512 : panel * 256));
  };
  auto chk2 = [&](int k) __attribute__((always_inline)) {
    if (__builtin_expect(!hasf(), 0)) {
#pragma unroll 1
      for (int spin = 0; spin < SPIN_MAX; ++spin) {
        ld2(k);
        vm_tie<0>(fl);
        if (hasf()) break;
      }
    }
  };
  auto flag2 = [&]() {
    u32x2 f2 = {(unsigned)tag, (unsigned)(tag >> 32)};
    unsigned td = tid; tie(td); tie(f2);
    __builtin_amdgcn_raw_buffer_store_b64(f2, rWS, td ? 0x7ff00000u : 0u, WS_F2 + (rt * 16 + cg) * 8, 16);
    SB();
  };
  {
    const unsigned gp = ((g & 1) << 1) | (g >> 1);
    const unsigned m2 = (w >> 2) * 2 + (w & 1), kh = (w >> 1) & 1;
    u32x4 xr;
    ld16(xr, (m2 * 16 + l15) * 1024 + gp * 16, rX, rt * 65536 + cg * 128 + kh * 64);
    bf16x8 wf[8][4];
#pragma unroll
    for (int ks = 0; ks < 8; ++ks)
#pragma unroll
      for (int c = 0; c < 4; ++c) {
        const unsigned a = kh * 32768 + trowN + ((c ^ (2 * kh) ^ frN) << 5) + ks * 4096;
        wf[ks][c] = lds_tr2(a, a + 512);
      }
    SB();
    vm_tie<0>(xr);
    publish(ws, f1, tag, tid);
    rdq(0);
#pragma unroll
    for (int i = 0; i < 8; ++i) dma_q(1, i);
    if (w == 0) poll(ws, WS_F1 + rt * 128, 8, tag, lane);
    wait_vm<0>();
    bar();
    u32x4 of[8];
    const unsigned ob = WS_O + ((rt * 4 + m2) * 16 + kh * 8) * 1024;
    ld16c<0>(of[0], lane * 16, rWS, ob); ld16c<1024>(of[1], lane * 16, rWS, ob); ld16c<2048>(of[2], lane * 16, rWS, ob); ld16c<3072>(of[3], lane * 16, rWS, ob);
    ld16c<0>(of[4], lane * 16, rWS, ob + 4096); ld16c<1024>(of[5], lane * 16, rWS, ob + 4096); ld16c<2048>(of[6], lane * 16, rWS, ob + 4096); ld16c<3072>(of[7], lane * 16, rWS, ob + 4096);
    rdq(1);
    csum(0, 8);
    f32x4 acc[4];
#pragma unroll
    for (int c = 0; c < 4; ++c) acc[c] = {0, 0, 0, 0};
#pragma unroll
    for (int ks = 0; ks < 8; ++ks) {
      vm_tie<7>(of[ks]);
#pragma unroll
      for (int c = 3; c >= 0; --c) acc[c] = mfma32(wf[ks][c], asbf(of[ks]), acc[c]);
      SB();
      dma_q(2, ks);
    }
    *(LDSP f32x4*)(smem + L_RED + w * 2048 + lane * 16) = acc[2];
    *(LDSP f32x4*)(smem + L_RED + w * 2048 + 1024 + lane * 16) = acc[3];
    ld2(0);
    bar();
    const f32x4 r0 = *(LDSP f32x4*)(smem + L_RED + (w ^ 2) * 2048 + lane * 16);
    const f32x4 r1 = *(LDSP f32x4*)(smem + L_RED + (w ^ 2) * 2048 + 1024 + lane * 16);
    SB();
#pragma unroll
    for (int i = 0; i < 2; ++i) dma_q(3, i);
    SB();
    const f32x4 k0 = acc[0] + r0, k1 = acc[1] + r1;
    const u32x4 av = swap_pair(pk(k0[0], k0[1]), pk(k0[2], k0[3]), pk(k1[0], k1[1]), pk(k1[2], k1[3]));
    u32x4 hreg;
#pragma unroll
    for (int j = 0; j < 4; ++j) hreg[j] = min(pk(blo(av[j]) + blo(xr[j]), bhi(av[j]) + bhi(xr[j])), ~1u);
    st16<16>(hreg, rWS, (gp * 16 + l15) * 16, WS_H + ((rt * 4 + m2) * 16 + cg * 2 + kh) * 1024);
    float s1 = 0.f, s2 = 0.f;
#pragma unroll
    for (int j = 0; j < 4; ++j) { s1 = dot2(hreg[j], 0x3f803f80u, s1); s2 = dot2(hreg[j], hreg[j], s2); }
    const u32x2 ss = {__float_as_uint(sum2rows(s1, s2)), (unsigned)(tag >> 32)};
    __builtin_amdgcn_raw_buffer_store_b64(ss, rWS, g < 2 ? l15 * 16 + g * 8 : 0x7ff00000u, WS_S2 + ((rt * 4 + m2) * 16 + cg * 2 + kh) * 256, 16);
    SB();
    vm_tie<4>(fl);
    bar();
  }

  const rsrc_t rD = mk(wd, D_ * F_ * 2);
  const unsigned vD0 = prow * 1024 + cg * 128 + ((((pp >> 1) ^ ((prow >> 1) & 1)) << 5) | ((pp & 1) << 4)), vD1 = vD0 ^ 64;
  const unsigned lw = w * 1024, sD = w * (7 * 32 * 1024);
  auto dma_wd = [&](int j, int i, unsigned area) { dmai<0>(lw, area + i * 8192, (i & 1) ? vD1 : vD0, rD, sD, (j * 32 + i * 8) * 1024); };
  bf16x8 wdf[7][4];
  unsigned tw;
  auto mktw = [&](unsigned base) {
    unsigned ln = lane; tie(ln);
    tw = base + lw + (ln >> 4) * 8192 + ((ln >> 2) & 3) * 128 + (ln & 3) * 8 + ((((ln >> 3) & 1) | ((ln >> 3) & 2)) << 5);
    tie(tw);
  };
  auto rdw = [&](int j, int c, unsigned area) {
    unsigned a = tw ^ (c << 5);
    tie(a);
    wdf[j][c] = lds_tr2(a + (area & 65535), a + (area & 65535) + 512);
  };
  auto tiew = [&](int j) { asm volatile("" : "+v"(wdf[j][0]), "+v"(wdf[j][1]), "+v"(wdf[j][2]), "+v"(wdf[j][3])); };
  auto ldf = [&](int k) { unsigned ln = lane; tie(ln); ld8c<0>(fl, min(ln, 13u) * 8, rWS, WS_F3 + k * 16384 + (rt >> 1) * 1024 + w * 112); };
  auto chkf = [&](int k) __attribute__((always_inline)) {
    if (__builtin_expect(!__all(fl[0] == (unsigned)tag && fl[1] == (unsigned)(tag >> 32)), 0)) {
#pragma unroll 1
      for (int spin = 0; spin < SPIN_MAX; ++spin) {
        __builtin_amdgcn_s_sleep(2);
        ldf(k);
        vm_tie<0>(fl);
        if (__all(fl[0] == (unsigned)tag && fl[1] == (unsigned)(tag >> 32))) break;
      }
    }
  };
  const unsigned ab4 = WS_A + (rt * 4 * 56 + w * 7) * 1024;
  u32x4 af[2][7];
  auto lda = [&](int t, int j) __attribute__((always_inline)) {
    const unsigned so = ab4 + t * (56 * 1024) + (j >> 2) * 4096;
    u32x4& a = af[t & 1][j];
    if ((j & 3) == 0) ld16c<0>(a, lane * 16, rWS, so); else if ((j & 3) == 1) ld16c<1024>(a, lane * 16, rWS, so);
    else if ((j & 3) == 2) ld16c<2048>(a, lane * 16, rWS, so); else ld16c<3072>(a, lane * 16, rWS, so);
  };
  unsigned hv[4];
  auto ldx = [&](int t) {
    unsigned ln = lane; tie(ln);
    const unsigned fc = ln >> 4, fq = (ln >> 2) & 3, fr = (ln >> 1) & 1, fp = ln & 1;
    ld4c(hv[t], (fc >> 1) * 1024 + ((fc & 1) * 2 + (fq >> 1)) * 256 + fr * 16 + (fq & 1) * 8 + fp * 4, rWS, WS_H + ((rt * 4 + t) * 16 + cg * 2) * 1024 + w * 32);
  };
  {
    const unsigned r2 = (lane >> 4) & 1, hk = lane >> 5;
    rdq(2);
#pragma unroll
    for (int i = 2; i < 8; ++i) {
      dma_q(3, i);
      csum(8 + 16 * (i - 2) / 6, 8 + 16 * (i - 1) / 6);
    }
    unsigned vH = r2 * 65536 + hk * 256 + l15 * 16 + off3, vs = g * 1024 + l15 * 16;
    tie(vH); tie(vs);
    chk2(0);
    wait_vm<0>();
    bar();
    u32x4 sp[4];
    const unsigned hb = WS_H + panel * 8 * 16384 + w * 512;
    auto dma_h = [&](int t, int i) { dmai<1>(lw, harea(t) + i * 8192, vH, rWS, hb, t * 16384 + i * 4096); };
    const unsigned so = WS_S2 + (panel * 8 + w) * 4096;
    auto lds2 = [&](int i) __attribute__((always_inline)) {
      if (i == 0) ld16c<0>(sp[0], vs, rWS, so); else if (i == 1) ld16c<256>(sp[1], vs, rWS, so);
      else if (i == 2) ld16c<512>(sp[2], vs, rWS, so); else ld16c<768>(sp[3], vs, rWS, so);
    };
    unsigned no;
    f32x2 me;
    auto stats = [&](int k) __attribute__((always_inline)) {
      if (k == 0) {
        const unsigned tg = (unsigned)(tag >> 32);
        no = ((sp[0][1] ^ tg) | (sp[0][3] ^ tg)) | ((sp[1][1] ^ tg) | (sp[1][3] ^ tg));
        no |= ((sp[2][1] ^ tg) | (sp[2][3] ^ tg)) | ((sp[3][1] ^ tg) | (sp[3][3] ^ tg));
        tie(no);
      } else if (k == 1) {
#pragma unroll
        for (int j = 0; j < 2; ++j)
          me[j] = add1(add1(__uint_as_float(sp[0][2 * j]), __uint_as_float(sp[1][2 * j])), add1(__uint_as_float(sp[2][2 * j]), __uint_as_float(sp[3][2 * j])));
        tie(me);
      } else if (k == 2) {
        const float r = sum2rows(me[0], me[1]) * (1.f / 512.f);
        me = swp16(r, r);
        tie(me);
      } else if (k == 3) {
        me[1] = __builtin_amdgcn_rsqf(fmaxf(me[1] - me[0] * me[0], 0.f) + 1e-5f);
        tie(me);
      } else {
        const float m1 = blo(pk(-me[0], 0.f));
        const u32x2 st = {pk(m1, -me[0] - m1), __float_as_uint(me[1] * -1.44269504f)};
        unsigned ln = lane; tie(ln);
        *lds<u32x2>(L_ST + ((w & 3) * 32 + (w >> 2) * 16 + (ln & 15)) * 8) = st;
      }
    };
    auto back = [&](int i0, int i1) __attribute__((always_inline)) {
      unsigned a = lane * 16 + lw + harea(0) + i0 * 8192, m = 0;
      tie(a);
#pragma unroll
      for (int i = i0; i < i1; ++i) m = max4(*lds<u32x4>(a + (i - i0) * 8192), m);
      return m;
    };
    const unsigned ones3 = bi < 28 ? ~0u : 0u;
    auto mend = [&](unsigned m, int i0, int i1) __attribute__((always_inline)) {
      if (__builtin_expect(__any((m & ones3) == ~0u), 0)) {
#pragma unroll 1
        for (int spin = 0; spin < SPIN_MAX; ++spin) {
#pragma unroll
          for (int i = i0; i < i1; ++i) dma_h(i >> 2, i & 3);
          wait_vm<0>();
          if (!__any(back(i0, i1) == ~0u)) break;
        }
      }
    };
#pragma unroll
    for (int i = 0; i < 5; ++i) {
      if (i == 2) rdq(3);
      dma_h(i >> 2, i & 3);
    }
    flag2();
    csum(24, 32);
    u32x2 ax;
    {
      const unsigned c1 = pk(cs[0], cs[0]);
      const float d = cs[0] - blo(c1);
      ax = {hk ? pk(d, d) : c1, 0u};
      tie(ax);
    }
    unsigned ao = mkao();
    unsigned ha[3] = {lane * 16u, lane * 16u + 65536, lane * 16u + 131072};
    unsigned ra = lane * 16 + lw + harea(0);
    tie(ha[1]); tie(ra);
    SB();
    wait_vm<4>();
    mend(back(0, 2), 0, 2);
    bar();
    bf16x8 hf[NW];
    auto rdh = [&](int f) { const unsigned a = harea(f >> 5) + (f & 31) * 1024; hf[f % NW] = lds_b128(ha[a >> 16] + (a & 65535)); };
    u32x2 st[4];
#pragma unroll
    for (int f = 0; f < NW; ++f) rdh(f);
    SB();
    f32x16 acc[2];
    float kk;
    u32x4 av;
    auto epi = [&](int t, int k) {
      f32x16& a = acc[t & 1];
      const float sc = __uint_as_float(st[t][1]);
      if (k == 0) kk = __builtin_amdgcn_rcpf(sc);
      else if (k == 1) kk = mul1(kk, 1.44269504f * 1.44269504f);
      else {
        const int i = (k - 2) / 7, o = (k - 2) % 7;
        if (o == 0) { if (i < 8) a[i] = mul1(a[i], sc); }
        else if (o == 1) { if (i >= 1 && i < 9) a[i - 1] = fma1(a[i - 1], kk, kk); }
        else if (o == 2) { if (i < 8) a[8 + i] = mul1(a[i], a[8 + i]); }
        else if (o == 3) { if (i < 8) { float e = __builtin_amdgcn_exp2f(a[i]); tie(e); a[i] = e; } }
        else if (o == 4) { if (i >= 2) a[i - 2] = mul1(a[i - 2], a[8 + i - 2]); }
        else if (o == 5) { if (i >= 1 && i < 9) { float e = __builtin_amdgcn_rcpf(a[i - 1]); tie(e); a[i - 1] = e; } }
        else if (i >= 3 && (i & 1)) { unsigned v = min(pk(a[i - 3], a[i - 2]), ~1u); tie(v); av[(i - 3) >> 1] = v; }
      }
    };
    constexpr int NE = 72;
    u32x4 rb[4];
    unsigned rm;
    PRIO(1);
    sfor<0, 4 * 33>([&](auto P) __attribute__((always_inline)) {
      constexpr int pos = P.value, t = pos / 33, n = pos % 33, op = vm_at(pos), rd = rd_at(pos);
      if constexpr (n == 1) PRIO(3);
      if constexpr (n == 12) PRIO(2);
      if constexpr (n == 23) PRIO(1);
      const f32x16 z = {0, 0, 0, 0, 0, 0, 0, 0, 0, 0, 0, 0, 0, 0, 0, 0};
      if constexpr (t == 1 && (n == 3 || n == 4)) {
        const u32x2 bx = {st[n - 3][0], st[n - 3][0]};
        acc[n - 3] = mfmaS(ax, bx, acc[n - 3]);
        tie(acc[n - 3]);
        SB();
      }
      if constexpr (n == 0) {
        if constexpr (t > 1) {
          const u32x2 bx = {st[t][0], st[t][0]};
          acc[t & 1] = mfmaS(ax, bx, z);
          tie(acc[t & 1]);
          SB();
        }
        if constexpr (t > 0) {
          wait_vm<younger(t == 3 ? oH(3, 3) : oH(t + 1, 0), pos)>();
          if constexpr (t == 1) asm volatile("s_waitcnt lgkmcnt(2)" ::: "memory");
          if constexpr (t == 2) tie(st[3]);
          asm volatile("s_barrier" ::: "memory");
        }
        if constexpr (t == 1) {
          unsigned sa = L_ST + (lane & 31) * 8; tie(sa);
#pragma unroll
          for (int i = 0; i < 4; ++i) st[i] = *lds<u32x2>(sa + i * 256);
        }
        if constexpr (t == 1) tie(ha[2]);
        if constexpr (t == 2) tie(ha[0]);
      } else {
        constexpr int f = t * 32 + n - 1;
        acc[t & 1] = mfmaL(fw[n - 1], hf[f % NW], pos == 1 || pos == 34 ? z : acc[t & 1]);
        tie(acc[t & 1]);
        SB();
        if constexpr (f + NW < 128) rdh(f + NW);
      }
      SB();
      constexpr int e0 = t == 1 ? 4 : 2, eg = 18;
      if constexpr (t > 0 && n >= e0 && n < e0 + eg) {
#pragma unroll
        for (int k = NE * (n - e0) / eg; k < NE * (n - e0 + 1) / eg; ++k) epi(t - 1, k);
      }
      if constexpr (t == 0) {
        constexpr int B1 = 16 - NW, G2 = B1 - 3;
        if constexpr (n == G2) {
          wait_vm<1 + before(pos)>();
#pragma unroll
          for (int i = 2; i < 5; ++i) rb[i - 1] = *lds<u32x4>(ra + i * 8192);
        }
        if constexpr (n == B1) {
          rm = 0;
#pragma unroll
          for (int i = 2; i < 5; ++i) rm = max4(rb[i - 1], rm);
          mend(rm, 2, 5);
          asm volatile("s_barrier" ::: "memory");
        }
        if constexpr (n == 20) {
          asm volatile("s_waitcnt vmcnt(%4)" : "+v"(sp[0]), "+v"(sp[1]), "+v"(sp[2]), "+v"(sp[3]) : "n"(younger(oZ(3), pos)) : "memory");
        }
        if constexpr (n >= 20 && n < 25) stats(n - 20);
        if constexpr (n == 21) {
          if (__builtin_expect(__any(no != 0), 0)) {
#pragma unroll 1
            for (int spin = 0; spin < SPIN_MAX; ++spin) {
              lds2(0); lds2(1); lds2(2); lds2(3);
              asm volatile("s_waitcnt vmcnt(0)" : "+v"(sp[0]), "+v"(sp[1]), "+v"(sp[2]), "+v"(sp[3]) :: "memory");
              stats(0);
              if (!__any(no != 0)) break;
            }
            stats(1);
          }
        }
      }
      SB();
      if constexpr (rd >= 0) {
        if constexpr ((rd & 3) == 0) { wait_vm<younger(oW(rd >> 2, 3), pos)>(); if (rd != 4 && rd != 16) mktw(warea(rd >> 2) & ~65535u); }
        rdw(rd >> 2, rd & 3, warea(rd >> 2));
      }
      if constexpr (op == oS) st16<16>(av, rWS, ao, ab + (t - 1) * (56 * 1024));
      else if constexpr (op == oF || op == oG) ldf(op == oG);
      else if constexpr (op == oE) ld2(1);
      else if constexpr (op == oP || op == oQ) {
        static_assert(younger(oC(2), 101) >= younger(oS, 101) && younger(oC(3), 101) >= younger(oS, 101));
        wait_vm<younger(oS, pos)>();
        const bool ep = last && op == oP && w == 0;
        const u64 fv = ep ? epoch + 1 : tag;
        u32x2 f2 = {(unsigned)fv, (unsigned)(fv >> 32)};
        unsigned ln = lane; tie(ln); tie(f2);
        __builtin_amdgcn_raw_buffer_store_b64(f2, rWS, ln ? 0x7ff00000u : ep ? 0u : off3, ep ? WS_EPOCH : WS_F3 + (op == oQ) * 16384 + (panel * 128 + fg * 8 + w) * 8, 16);
      }
      else if constexpr (op == oT) {
        unsigned ln = lane; tie(ln);
        touch_l(harea(3) + lw, ln < 5 ? ln * 1024 : 0x7f000000u, rD, ((b >> 5) * 224 + 64 + (b & 31) * 5) * 1024 + w * 128);
      }
      else if constexpr (op >= oA(0, 0)) lda((op - 64) >> 3, op & 7);
      else if constexpr (op >= oZ(0)) lds2(op - oZ(0));
      else if constexpr (op >= oW(0, 0)) {
        constexpr int j = (op - 32) >> 2;
        if constexpr ((op & 3) == 0 && (j == 1 || j == 2 || j == 6)) tiew(j == 6 ? 2 : j - 1);
        dma_wd(j, op & 3, warea(j));
      }
      else if constexpr (op >= oH(0, 0)) dma_h((op - 16) >> 2, op & 3);
      else if constexpr (op >= oC(0)) {
        if constexpr (op == oC(2)) { av = {~0u, ~0u, ~0u, ~0u}; tie(av); }
        st16<16>(av, rWS, ao, ab + (op - oC(0)) * (56 * 1024));
      }
      else if constexpr (op >= oX(0)) ldx(op - 8);
      if constexpr (pos == 22) {
        static_assert(vm_at(23) == oH(2, 1) && younger(oE, 23) < younger(oH(2, 0), 23));
        vm_tie<younger(oE, pos + 1)>(fl);
        chk2(1);
      }
      if constexpr (pos >= 24 && pos < 28) rb[pos - 24] = *lds<u32x4>(ra + (pos - 19) * 8192);
      if constexpr (pos >= 27 && pos < 31) { rm = max4(rb[pos - 27], pos == 27 ? 0u : rm); tie(rm); }
      if constexpr (pos == 31) mend(rm, 5, 9);
      if constexpr (pos == CHK) {
        vm_tie<younger(oF, pos + 1)>(fl);
        chkf(0);
      }
      SB();
    });
    PRIO(3);
    f32x4 ac[2][4];
    unsigned pq[2], pr, vOut, q0, q1;
    u64 bad;
    f32x2 vv[2][9];
    const rsrc_t rOut = mk(out, S_ * D_ * 2);
    auto put = [&](int t, int c) { *lds<f32x4>(pq[c & 1] + (t & 1) * 32768 + c * 8192) = ac[t & 1][c]; };
    auto fin = [&](int t, int k) __attribute__((always_inline)) {
      auto& v = vv[t == 3];
      if (k < 4) {
        v[2 * k] = *lds<f32x2>(pr + (t & 1) * 32768 + k * 2048);
        asm volatile("" ::: "memory");
        v[2 * k + 1] = *lds<f32x2>(pr + (t & 1) * 32768 + k * 2048 + 1024);
        asm volatile("" ::: "memory");
      } else if (k < 11) {
        constexpr int A[7] = {0, 2, 4, 6, 0, 4, 0}, B[7] = {1, 3, 5, 7, 2, 6, 4};
        v[A[k - 4]] = {add1(v[A[k - 4]][0], v[B[k - 4]][0]), add1(v[A[k - 4]][1], v[B[k - 4]][1])};
      } else if (k == 11) {
        v[8] = {blo(hv[t]), bhi(hv[t])};
        tie(v[8]);
      } else if (k == 12) {
        const unsigned f = pk(v[0][0], v[0][1]);
        v[0] = {blo(f), bhi(f)};
        tie(v[0]);
      } else if (k == 13) {
        hv[t] = pk(add1(v[0][0], v[8][0]), add1(v[0][1], v[8][1]));
        tie(hv[t]);
      } else if (k == 14) __builtin_amdgcn_raw_buffer_store_b32(hv[t], rOut, vOut, (rt * 64 + t * 16 + 2 * w) * 1024 + cg * 128, 19);
    };
    auto again = [&](int t) __attribute__((always_inline)) {
#pragma unroll
      for (int j = 0; j < 7; ++j) lda(t, j);
      auto& a = af[t & 1];
      asm volatile("s_waitcnt vmcnt(0)" : "+v"(a[0]), "+v"(a[1]), "+v"(a[2]), "+v"(a[3]), "+v"(a[4]), "+v"(a[5]), "+v"(a[6]) :: "memory");
    };
    auto redo = [&](int t) __attribute__((always_inline)) {
      wait_vm<0>();
#pragma unroll 1
      for (int spin = 0; spin < SPIN_MAX; ++spin) {
        again(t);
        unsigned r = 0;
#pragma unroll
        for (int i = 0; i < 28; ++i) r = max(r, af[t & 1][i >> 2][i & 3]);
        if (!__any(r == ~0u)) break;
        __builtin_amdgcn_s_sleep(2);
      }
#pragma unroll
      for (int i = 0; i < 7; ++i) mstep(ac[t & 1], wdf[i], af[t & 1][i], i);
      asm volatile("s_nop 7" : "+v"(ac[t & 1][0]), "+v"(ac[t & 1][1]), "+v"(ac[t & 1][2]), "+v"(ac[t & 1][3]));
    };
    constexpr int AD = SM + 2 > 20 ? SM + 2 : 20;
    static_assert(AD + 2 <= putp(0, 0) && AD + 5 <= barp(0));
    sfor<0, 4 * 28>([&](auto P) __attribute__((always_inline)) {
      constexpr int p = P.value, t = p / 28, m = p % 28, pos = 132 + p, op = vm_at(pos);
      constexpr bool cm = t == 3 && m >= 12;
      constexpr int j = cm ? 3 + m % 4 : m / 4, c = cm ? m / 4 - 3 : m % 4;
      u32x4& a = af[t & 1][j];
      constexpr int nw = younger(oA(t, j), pos);
      if constexpr (c == 0 && j > 0) asm volatile("s_waitcnt vmcnt(%3)\n\t" MF "%0, %1, %2, %0" : "+v"(ac[t & 1][c]) : "v"(wdf[j][c]), "v"(a), "n"(nw));
      else if constexpr (c == 0) asm volatile("s_waitcnt vmcnt(%3)\n\t" MF "%0, %1, %2, 0" : "=&v"(ac[t & 1][c]) : "v"(wdf[j][c]), "v"(a), "n"(nw));
      else if constexpr (j > 0) asm volatile(MF "%0, %1, %2, %0" : "+v"(ac[t & 1][c]) : "v"(wdf[j][c]), "v"(a));
      else asm volatile(MF "%0, %1, %2, 0" : "=&v"(ac[t & 1][c]) : "v"(wdf[j][c]), "v"(a));
      SB();
      if constexpr (p == 84 || p == 110) asm volatile("v_cmp_u_f32 %0, %1, %1" : "=s"(bad) : "v"(ac[p > 90][0][0]));
      if constexpr (p == 0) mktw(131072);
      if constexpr (p == 1) {
        epi(3, 0); epi(3, 1);
        static_assert(younger(oW(6, 3), pos) >= younger(oG, pos));
        asm volatile("s_waitcnt vmcnt(%5)" : "+v"(hv[0]), "+v"(hv[1]), "+v"(hv[2]), "+v"(hv[3]), "+v"(fl) : "n"(younger(oG, pos)) : "memory");
      }
      if constexpr (p >= 2) {
#pragma unroll
        for (int k = 6 * p - 10; k < 6 * p - 4; ++k) if (k < NE) epi(3, k);
      }
      if constexpr (p >= 12 && p < 16) rdw(5, p - 12, warea(5));
      if constexpr (p == 16) tw -= 131072;
      if constexpr (p >= 16 && p < 20) rdw(6, p - 16, 0);
      if constexpr (p == 26) { bad = __builtin_amdgcn_ballot_w64(fl[0] != (unsigned)tag) | __builtin_amdgcn_ballot_w64(fl[1] != (unsigned)(tag >> 32)); asm volatile("" : "+s"(bad)); }
      {
        unsigned l16 = lane * 16;
        if constexpr (p == AD) { q0 = (l16 >> 3) & 96; tie(q0); }
        if constexpr (p == AD + 1) { pq[0] = (l16 ^ q0) + (lw + 65536); tie(pq[0]); pq[1] = pq[0] ^ 128; tie(pq[1]); }
        if constexpr (p == AD + 2) { pr = ((l16 >> 1) & 255) ^ (w * 32 + 65536); tie(pr); }
        if constexpr (p == AD + 3) { q0 = l16 & 192; tie(q0); pr += q0 * 4; tie(pr); }
        if constexpr (p == AD + 4) { q0 = l16 & 768; tie(q0); pr += q0 * 32; tie(pr); }
        if constexpr (p == AD + 5) { q0 = (l16 >> 4) & 3; tie(q0); q0 = __umul24(q0, 516); tie(q0); }
        if constexpr (p == AD + 6) { q0 &= 1028; tie(q0); q1 = l16 >> 6; tie(q1); }
        if constexpr (p == AD + 7) { vOut = q1 * 8 + q0; tie(vOut); }
      }
      sfor<0, 4>([&](auto U) __attribute__((always_inline)) {
        constexpr int u = U.value, d = p - barp(u);
        sfor<0, 4>([&](auto C) __attribute__((always_inline)) {
          if constexpr (p == putp(u, C.value)) {
            if constexpr (u == 3) asm volatile("s_nop 3" : "+v"(ac[1][C.value]));
            put(u, C.value);
          }
        });
        if constexpr (u < 3) {
          if constexpr (d == 0) bar();
          if constexpr (d >= 1 && d <= 4) fin(u, d - 1);
          if constexpr (u < 2 && d >= 7 && d < 17) fin(u, d - 3);
        }
      });
      SB();
      if constexpr (op == oS) { st16<16>(av, rWS, ao, ab + 3 * (56 * 1024)); PRIO(0); }
      else if constexpr (op == oO) fin(p > 84, 14);
      else if constexpr (op >= oA(0, 0)) lda((op - 64) >> 3, op & 7);
      SB();
      if constexpr (p == 27 || p == 85 || p == 111) {
        if (__builtin_expect(bad != 0, 0)) {
          if constexpr (p == 27) { wait_vm<0>(); chkf(1); again(1); }
          else if constexpr (p < 111) redo(2);
          else {
            redo(3);
#pragma unroll
            for (int c = 0; c < 4; ++c) if (putp(3, c) < 112) put(3, c);
          }
        }
        SB();
      }
    });
    static_assert(barp(2) + 4 < 112 && barp(2) < putp(3, 0) && barp(1) < putp(2, 0) && free_at(barp(1) + 18) <= barp(2));
#pragma unroll
    for (int k = 4; k < 8; ++k) fin(2, k);
    SB();
    asm volatile("" : "+v"(ac[1][0]), "+v"(ac[1][1]), "+v"(ac[1][2]), "+v"(ac[1][3]));
#pragma unroll
    for (int c = 0; c < 4; ++c) if (putp(3, c) >= 112) put(3, c);
    SB();
#pragma unroll
    for (int k = 8; k < 12; ++k) fin(2, k);
    SB();
    bar();
#pragma unroll
    for (int k = 0; k < 4; ++k) fin(3, k);
    SB();
#pragma unroll
    for (int k = 12; k < 15; ++k) fin(2, k);
    fin(3, 11);
    SB();
#pragma unroll
    for (int k = 4; k < 15; ++k) if (k != 11) fin(3, k);
  }
}

extern "C" void kernel_launch(void* const* d_in, const int* in_sizes, int n_in, void* d_out, int out_size, void* d_ws,
                              size_t ws_size, hipStream_t stream) {
  (void)hipFuncSetAttribute((const void*)fused_block, hipFuncAttributeMaxDynamicSharedMemorySize, LDS_BYTES);
  fused_block<<<256, 512, LDS_BYTES, stream>>>((const u16*)d_in[0], (const u16*)d_in[1], (const u16*)d_in[2], (const u16*)d_in[4],
                                               (const u16*)d_in[5], (const u16*)d_in[6], (u16*)d_out, (char*)d_ws);
}
```
